```python
import torch

def _softmax(x, axis=-1):
    return torch.softmax(x, dim=axis)
def _gelu(x):
    return torch.nn.functional.gelu(x, approximate="tanh")
def _silu(x):
    return torch.nn.functional.silu(x)
def _sigmoid(x):
    return torch.sigmoid(x)
def _layernorm(x, axis=-1, eps=1e-5):
    m = x.mean(dim=axis, keepdim=True)
    v = x.var(dim=axis, keepdim=True, unbiased=False)
    return (x - m) / torch.sqrt(v + eps)
def _scatter_add(tgt, idx, src, axis=0):
    return tgt.clone().index_add_(axis, idx, src)
def _segment_sum(src, seg, n):
    out = torch.zeros((n,) + src.shape[1:], dtype=src.dtype)
    return out.index_add_(0, seg, src)
def _pad(x, pw):
    flat = tuple(v for pair in reversed(pw) for v in pair)
    return torch.nn.functional.pad(x, flat)

kernel_name = 'sequential_preLN__mha_block_cached__dense_activation_gelu__decode'

def setup_inputs():

    _w = (lambda a: (lambda t: t.to(torch.bfloat16) if t.is_floating_point() else t)(torch.as_tensor(a)))
    def _A_setup():
        B, Sq, D = 2, 1, 512
        H, Sk, Dh = 32, 4096, 128
        return {
            "x": _w((lambda *s: torch.randn(*s, dtype=torch.bfloat16) * 0.1)(B, Sq, D)),
            "k_cache": _w((lambda *s: torch.randn(*s, dtype=torch.bfloat16) * 1.6)(B, H, Sk, Dh)),
            "v_cache": _w((lambda *s: torch.randn(*s, dtype=torch.bfloat16) * 1.6)(B, H, Sk, Dh)),
            "wq": _w((lambda *s: torch.randn(*s, dtype=torch.bfloat16) * 1.6)(D, H * Dh) * (D ** -0.5)),
            "wo": _w((lambda *s: torch.randn(*s, dtype=torch.bfloat16) * 1.6)(H * Dh, D) * ((H * Dh) ** -0.5)),
        }
    _A_in = _A_setup()

    def _B_setup():
        B, S, D, F = 2, 1, 512, 1376
        return {
            "x": _w((lambda *s: torch.randn(*s, dtype=torch.bfloat16) * 0.1)(B, S, D)),
            "w1": _w((lambda *s: torch.randn(*s, dtype=torch.bfloat16) * 3.2)(D, F) * (D ** -0.5)),
            "b1": _w((lambda *s: torch.randn(*s, dtype=torch.bfloat16) * 3.2)(F) * 0.01),
            "w2": _w((lambda *s: torch.randn(*s, dtype=torch.bfloat16) * 3.2)(F, D) * (F ** -0.5)),
            "b2": _w((lambda *s: torch.randn(*s, dtype=torch.bfloat16) * 3.2)(D) * 0.01),
        }
    _B_in = _B_setup()

    return {
        "A_x": _A_in["x"],
        "A_k_cache": _A_in["k_cache"],
        "A_v_cache": _A_in["v_cache"],
        "A_wq": _A_in["wq"],
        "A_wo": _A_in["wo"],
        "B_x": _B_in["x"],
        "B_w1": _B_in["w1"],
        "B_b1": _B_in["b1"],
        "B_w2": _B_in["w2"],
        "B_b2": _B_in["b2"],
    }

def reference(A_x, A_k_cache, A_v_cache, A_wq, A_wo, B_x, B_w1, B_b1, B_w2, B_b2):
    def _A_ref(x, k_cache, v_cache, wq, wo):
        B, Sq, D = x.shape
        H, Dh = 32, 128
        q = (lambda x, *d: x.permute(*d))((x @ wq).reshape(B, Sq, H, Dh), 0, 2, 1, 3)
        scores = torch.einsum("bhqd,bhkd->bhqk", q, k_cache) * (Dh ** -0.5)
        p = _softmax(scores, axis=-1)
        o = torch.einsum("bhqk,bhkd->bhqd", p, v_cache)
        return (lambda x, *d: x.permute(*d))(o, 0, 2, 1, 3).reshape(B, Sq, H * Dh) @ wo

    def _B_ref(x, w1, b1, w2, b2):
        h = torch.einsum("bsd,df->bsf", x, w1) + b1
        h = _gelu(h)
        return torch.einsum("bsf,fd->bsd", h, w2) + b2

    x = A_x
    h = x + _A_ref(_layernorm(x, axis=-1), A_k_cache, A_v_cache, A_wq, A_wo)
    h2 = h + _B_ref(_layernorm(h, axis=-1), B_w1, B_b1, B_w2, B_b2)
    return h2
```

```cpp
#include <hip/hip_runtime.h>
#include <hip/hip_bf16.h>
#include <stdint.h>

typedef unsigned short u16;
typedef unsigned u32x4 __attribute__((ext_vector_type(4)));
typedef __bf16 bf16x2 __attribute__((ext_vector_type(2)));

constexpr int B = 2, D = 512, H = 32, SK = 4096, DH = 128, F = 1376;
constexpr int NS = 4;

__device__ __forceinline__ float bf2f(u16 v) { return __uint_as_float(((unsigned)v) << 16); }
__device__ __forceinline__ float lo(unsigned v) { return __uint_as_float(v << 16); }
__device__ __forceinline__ float hi(unsigned v) { return __uint_as_float(v & 0xffff0000u); }
__device__ __forceinline__ u16 f2bf(float f) { __hip_bfloat16 b = __float2bfloat16(f); return *reinterpret_cast<u16*>(&b); }

__device__ float block_sum(float v, float* red) {
  for (int o = 32; o > 0; o >>= 1) v += __shfl_xor(v, o);
  int w = threadIdx.x >> 6, nw = blockDim.x >> 6;
  __syncthreads();
  if ((threadIdx.x & 63) == 0) red[w] = v;
  __syncthreads();
  float s = 0;
  for (int i = 0; i < nw; ++i) s += red[i];
  return s;
}

__global__ void k_q(const u16* x, const u16* wq, float* q) {
  __shared__ float xn[B][D];
  __shared__ float red[16];
  __shared__ float part[4][B][128];
  int t = threadIdx.x;
  for (int b = 0; b < B; ++b) {
    float v = bf2f(x[b * D + t]);
    float m = block_sum(v, red) / D;
    float d = v - m;
    float var = block_sum(d * d, red) / D;
    xn[b][t] = d / sqrtf(var + 1e-5f);
  }
  __syncthreads();
  int col = blockIdx.x * 128 + (t & 127), g = t >> 7;
  float a0 = 0, a1 = 0;
  for (int k = g * 128; k < g * 128 + 128; ++k) {
    float w = bf2f(wq[(size_t)k * (H * DH) + col]);
    a0 += xn[0][k] * w; a1 += xn[1][k] * w;
  }
  part[g][0][t & 127] = a0; part[g][1][t & 127] = a1;
  __syncthreads();
  if (t < 256) {
    int b = t >> 7, c = t & 127;
    q[b * H * DH + blockIdx.x * 128 + c] = part[0][b][c] + part[1][b][c] + part[2][b][c] + part[3][b][c];
  }
}

__global__ void k_attn(const float* q, const u16* kc, const u16* vc, float* po, float* pml) {
  __shared__ float sc[SK / NS];
  __shared__ float red[16];
  __shared__ float acc_s[4][4][128];
  int s = blockIdx.x, h = blockIdx.y, b = blockIdx.z, t = threadIdx.x, lane = t & 63, w = t >> 6, r = lane >> 4, c = lane & 15;
  const int KS = SK / NS;
  const u16* kb = kc + ((size_t)(b * H + h) * SK + s * KS) * DH;
  const u16* vb = vc + ((size_t)(b * H + h) * SK + s * KS) * DH;
  float qv[8];
  for (int i = 0; i < 8; ++i) qv[i] = q[(b * H + h) * DH + 8 * c + i] * 0.08838834764831845f;
  for (int k0 = w * 4; k0 < KS; k0 += 16) {
    u32x4 kv = *reinterpret_cast<const u32x4*>(kb + (size_t)(k0 + r) * DH + 8 * c);
    float d = 0;
    for (int i = 0; i < 4; ++i) d += lo(kv[i]) * qv[2 * i] + hi(kv[i]) * qv[2 * i + 1];
    for (int o = 8; o > 0; o >>= 1) d += __shfl_xor(d, o);
    if (c == 0) sc[k0 + r] = d;
  }
  __syncthreads();
  float m = -INFINITY;
  for (int i = t; i < KS; i += 256) m = fmaxf(m, sc[i]);
  for (int o = 32; o > 0; o >>= 1) m = fmaxf(m, __shfl_xor(m, o));
  if (lane == 0) red[w] = m;
  __syncthreads();
  m = fmaxf(fmaxf(red[0], red[1]), fmaxf(red[2], red[3]));
  float l = 0;
  for (int i = t; i < KS; i += 256) { float p = __expf(sc[i] - m); sc[i] = p; l += p; }
  l = block_sum(l, red);
  float acc[8] = {0, 0, 0, 0, 0, 0, 0, 0};
  for (int k0 = w * 4; k0 < KS; k0 += 16) {
    u32x4 vv = *reinterpret_cast<const u32x4*>(vb + (size_t)(k0 + r) * DH + 8 * c);
    float p = sc[k0 + r];
    for (int i = 0; i < 4; ++i) { acc[2 * i] += p * lo(vv[i]); acc[2 * i + 1] += p * hi(vv[i]); }
  }
  for (int i = 0; i < 8; ++i) acc_s[w][r][8 * c + i] = acc[i];
  __syncthreads();
  if (t < 128) {
    float o = 0;
    for (int i = 0; i < 4; ++i) for (int j = 0; j < 4; ++j) o += acc_s[i][j][t];
    po[((b * H + h) * NS + s) * DH + t] = o;
  }
  if (t == 0) { pml[((b * H + h) * NS + s) * 2] = m; pml[((b * H + h) * NS + s) * 2 + 1] = l; }
}

__global__ void k_oproj(const float* po, const float* pml, const u16* wo, float* yp) {
  __shared__ float o[B][DH];
  int h = blockIdx.x, t = threadIdx.x;
  if (t < 256) {
    int b = t >> 7, d = t & 127;
    float M = -INFINITY;
    for (int s = 0; s < NS; ++s) M = fmaxf(M, pml[((b * H + h) * NS + s) * 2]);
    float L = 0, a = 0;
    for (int s = 0; s < NS; ++s) {
      float w = __expf(pml[((b * H + h) * NS + s) * 2] - M);
      L += w * pml[((b * H + h) * NS + s) * 2 + 1];
      a += w * po[((b * H + h) * NS + s) * DH + d];
    }
    o[b][d] = a / L;
  }
  __syncthreads();
  float a0 = 0, a1 = 0;
  for (int d = 0; d < DH; ++d) {
    float w = bf2f(wo[(size_t)(h * DH + d) * D + t]);
    a0 += o[0][d] * w; a1 += o[1][d] * w;
  }
  yp[(h * B + 0) * D + t] = a0; yp[(h * B + 1) * D + t] = a1;
}

__global__ void k_ffn(const u16* x, const float* yp, const u16* w1, const u16* b1, const u16* w2, float* fp, float* hout) {
  __shared__ float hn[B][D];
  __shared__ float red[16];
  __shared__ float part[16][B][32];
  __shared__ float g[B][32];
  int t = threadIdx.x;
  for (int b = 0; b < B; ++b) {
    float v = bf2f(x[b * D + t]);
    float a = 0;
    for (int h = 0; h < H; ++h) a += yp[(h * B + b) * D + t];
    v += a;
    if (blockIdx.x == 0) hout[b * D + t] = v;
    float m = block_sum(v, red) / D;
    float d = v - m;
    float var = block_sum(d * d, red) / D;
    hn[b][t] = d / sqrtf(var + 1e-5f);
  }
  __syncthreads();
  int f = blockIdx.x * 32 + (t & 31), kg = t >> 5;
  float a0 = 0, a1 = 0;
  for (int k = kg * 32; k < kg * 32 + 32; ++k) {
    float w = bf2f(w1[(size_t)k * F + f]);
    a0 += hn[0][k] * w; a1 += hn[1][k] * w;
  }
  part[kg][0][t & 31] = a0; part[kg][1][t & 31] = a1;
  __syncthreads();
  if (t < 64) {
    int b = t >> 5, c = t & 31;
    float z = bf2f(b1[blockIdx.x * 32 + c]);
    for (int i = 0; i < 16; ++i) z += part[i][b][c];
    float u = 0.7978845608028654f * (z + 0.044715f * z * z * z);
    g[b][c] = 0.5f * z * (1.f + tanhf(u));
  }
  __syncthreads();
  a0 = 0; a1 = 0;
  for (int i = 0; i < 32; ++i) {
    float w = bf2f(w2[(size_t)(blockIdx.x * 32 + i) * D + t]);
    a0 += g[0][i] * w; a1 += g[1][i] * w;
  }
  fp[(blockIdx.x * B + 0) * D + t] = a0; fp[(blockIdx.x * B + 1) * D + t] = a1;
}

__global__ void k_final(const float* fp, const float* hin, const u16* b2, u16* out) {
  int i = blockIdx.x * blockDim.x + threadIdx.x;
  int b = i >> 9, c = i & 511;
  float a = bf2f(b2[c]);
  for (int j = 0; j < 43; ++j) a += fp[(j * B + b) * D + c];
  out[i] = f2bf(hin[i] + a);
}

extern "C" void kernel_launch(void* const* d_in, const int* in_sizes, int n_in,
                              void* d_out, int out_size, void* d_ws, size_t ws_size,
                              hipStream_t stream) {
  const u16* x = (const u16*)d_in[0];
  const u16* kc = (const u16*)d_in[1];
  const u16* vc = (const u16*)d_in[2];
  const u16* wq = (const u16*)d_in[3];
  const u16* wo = (const u16*)d_in[4];
  const u16* w1 = (const u16*)d_in[6];
  const u16* b1 = (const u16*)d_in[7];
  const u16* w2 = (const u16*)d_in[8];
  const u16* b2 = (const u16*)d_in[9];
  float* ws = (float*)d_ws;
  float* q = ws;
  float* po = q + 8192;
  float* pml = po + 32768;
  float* yp = pml + 512;
  float* fp = yp + 32768;
  float* hb = fp + 43 * 1024;
  k_q<<<32, 512, 0, stream>>>(x, wq, q);
  k_attn<<<dim3(NS, H, B), 256, 0, stream>>>(q, kc, vc, po, pml);
  k_oproj<<<32, 512, 0, stream>>>(po, pml, wo, yp);
  k_ffn<<<43, 512, 0, stream>>>(x, yp, w1, b1, w2, fp, hb);
  k_final<<<4, 256, 0, stream>>>(fp, hb, b2, (u16*)d_out);
}
```

```cpp
#include <hip/hip_runtime.h>
#include <stdint.h>

typedef unsigned short u16;
typedef unsigned int u32;
typedef u32 u32x4 __attribute__((ext_vector_type(4)));
typedef u32 u32x2 __attribute__((ext_vector_type(2)));
typedef float f32x4 __attribute__((ext_vector_type(4)));
typedef float f32x2 __attribute__((ext_vector_type(2)));
typedef __bf16 bf16x2 __attribute__((ext_vector_type(2)));
typedef __amdgpu_buffer_rsrc_t rsrc_t;

#define WIN 16
#define KPRE 12
#define QPOS 20
#define STAMPS 0

constexpr int NB = 2, DM = 512, NH = 32, SK = 4096, DH = 128, FF = 1376;
constexpr int NFW = 43;
constexpr int AUX_NT = 2, AUX_SC1 = 16;
constexpr u32 OOB = 0x7ffffff0u;
constexpr u32 WS_E = 0;
constexpr u32 WS_Q = 4096;
constexpr u32 WS_ML = WS_Q + 32 * 8 * 2 * 64 * 16;
constexpr u32 WS_H = WS_ML + 256 * 64;
constexpr u32 WS_Y = WS_H + 4096;
constexpr u32 WS_F = WS_Y + 2 * 128 * 256 * 16;
constexpr u32 WS_END = WS_F + NFW * 2 * 256 * 16;
constexpr u32 WS_TS = 8u << 20;
constexpr u32 SPIN_CAP = 1u << 20;

#define SB() __builtin_amdgcn_sched_barrier(0)
#define STR2(x) #x
#define STR(x) STR2(x)
#define WAITVM_N(n) asm volatile("s_waitcnt vmcnt(%0)" ::"n"(n) : "memory")
#define LDS_SYNC() do { asm volatile("s_waitcnt lgkmcnt(0)" ::: "memory"); } while (0)
#define WG_BARRIER() do { asm volatile("s_waitcnt lgkmcnt(0)\n\ts_barrier" ::: "memory"); } while (0)

__device__ __forceinline__ float asf(u32 v) { return __builtin_bit_cast(float, v); }
__device__ __forceinline__ u32 asu(float v) { return __builtin_bit_cast(u32, v); }
__device__ __forceinline__ float bf_lo(u32 v) { return asf(v << 16); }
__device__ __forceinline__ float bf_hi(u32 v) { return asf(v & 0xffff0000u); }
__device__ __forceinline__ u32 pack_bf16(float lo, float hi) {
  return __builtin_bit_cast(u32, __builtin_convertvector(f32x2{lo, hi}, bf16x2));
}
__device__ __forceinline__ float rbf(float x) { return bf_lo(pack_bf16(x, x)); }
__device__ __forceinline__ float dot2(u32 a, u32 b, float c) {
  return __builtin_amdgcn_fdot2_f32_bf16(__builtin_bit_cast(bf16x2, a), __builtin_bit_cast(bf16x2, b), c, false);
}
template <int CTRL> __device__ __forceinline__ float dpp(float x) {
  return asf((u32)__builtin_amdgcn_mov_dpp((int)asu(x), CTRL, 0xf, 0xf, true));
}
template <int CTRL> __device__ __forceinline__ u32 dppu(u32 x) { return (u32)__builtin_amdgcn_mov_dpp((int)x, CTRL, 0xf, 0xf, true); }
constexpr int XOR1 = 0xB1, XOR2 = 0x4E, HMIR = 0x141, MIR = 0x140, ROR4 = 0x124, ROR8 = 0x128;
__device__ __forceinline__ float swap16_add(float a, float b) {
  auto r = __builtin_amdgcn_permlane16_swap(asu(a), asu(b), false, false);
  return asf(r[0]) + asf(r[1]);
}
__device__ __forceinline__ float swap32_add(float a, float b) {
  auto r = __builtin_amdgcn_permlane32_swap(asu(a), asu(b), false, false);
  return asf(r[0]) + asf(r[1]);
}
__device__ __forceinline__ float swap16_max(float a) {
  auto r = __builtin_amdgcn_permlane16_swap(asu(a), asu(a), false, false);
  return fmaxf(asf(r[0]), asf(r[1]));
}
__device__ __forceinline__ float swap32_max(float a) {
  auto r = __builtin_amdgcn_permlane32_swap(asu(a), asu(a), false, false);
  return fmaxf(asf(r[0]), asf(r[1]));
}
__device__ __forceinline__ float wave_sum(float x) {
  x += dpp<XOR1>(x); x += dpp<XOR2>(x); x += dpp<HMIR>(x); x += dpp<MIR>(x);
  x = swap16_add(x, x); x = swap32_add(x, x);
  return x;
}
__device__ __forceinline__ float wave_max(float x) {
  x = fmaxf(x, dpp<XOR1>(x)); x = fmaxf(x, dpp<XOR2>(x)); x = fmaxf(x, dpp<HMIR>(x)); x = fmaxf(x, dpp<MIR>(x));
  x = swap16_max(x); x = swap32_max(x);
  return x;
}
template <int CTRL> __device__ __forceinline__ float tr_step(float a, float b, bool sel) {
  const float keep = sel ? b : a, give = sel ? a : b;
  return keep + dpp<CTRL>(give);
}
__device__ __forceinline__ rsrc_t mk_rsrc(const void* p, u32 bytes) {
  return __builtin_amdgcn_make_buffer_rsrc(const_cast<void*>(p), (short)0, (int)bytes, 0x00020000);
}
template <int AUX> __device__ __forceinline__ u32x4 ld128(rsrc_t r, u32 voff, u32 soff) {
  return __builtin_amdgcn_raw_buffer_load_b128(r, (int)voff, (int)soff, AUX);
}
template <int AUX> __device__ __forceinline__ u32 ld32(rsrc_t r, u32 voff, u32 soff) {
  return __builtin_amdgcn_raw_buffer_load_b32(r, (int)voff, (int)soff, AUX);
}
__device__ __forceinline__ void st_unit(rsrc_t r, u32 off, u32 a, u32 b, u32 tag) {
  __builtin_amdgcn_raw_buffer_store_b128(u32x4{a, tag, b, tag}, r, (int)off, 0, AUX_SC1);
}
__device__ __forceinline__ bool unit_ok(u32x4 v, u32 tag) { return v[1] == tag && v[3] == tag; }
__device__ __forceinline__ float ex2(float x) { return __builtin_amdgcn_exp2f(x); }
constexpr float LOG2E = 1.4426950408889634f;

__device__ __forceinline__ void ln_stats(const float (&v)[8], float& mb, float& den) {
  float s = ((v[0] + v[1]) + (v[2] + v[3])) + ((v[4] + v[5]) + (v[6] + v[7]));
  const float mean = wave_sum(s) * (1.f / DM);
  float q = 0.f;
#pragma unroll
  for (int i = 0; i < 8; ++i) { const float d = v[i] - mean; q = fmaf(d, d, q); }
  const float var = wave_sum(q) * (1.f / DM);
  mb = rbf(mean);
  den = rbf(sqrtf(rbf(rbf(var) + 1e-5f)));
}
__device__ __forceinline__ float ln_apply(float x, float mb, float den) { return rbf(rbf(x - mb) / den); }

struct Lds {
  float qp[8][2][128];
  u32 qb[8][64];
  float p[8][4][32];
  float ml[8][2];
  float acc[8][128];
  float z[8][64];
  u32 g[8][32];
};

#define STAMP(k) do { } while (0)

__global__ void __launch_bounds__(512, 2)
fused_block(const u16* __restrict__ x, const u16* __restrict__ kc, const u16* __restrict__ vc, const u16* __restrict__ wq,
            const u16* __restrict__ wo, const u16* __restrict__ w1, const u16* __restrict__ b1, const u16* __restrict__ w2,
            const u16* __restrict__ b2, u16* __restrict__ out, unsigned char* __restrict__ ws) {
  __shared__ __attribute__((aligned(16))) Lds L;
  const int tid = threadIdx.x, lane = tid & 63, w = __builtin_amdgcn_readfirstlane(tid >> 6);
  const int r = lane >> 4, c = lane & 15, r8 = lane >> 3, c8 = lane & 7;
  const int bid = blockIdx.x, xcd = bid & 7, jj = bid >> 3;
  const int h = xcd * 4 + (jj >> 3), mem = jj & 7, b = mem >> 2, s = mem & 3;
  STAMP(0);
  const rsrc_t rws = mk_rsrc(ws, WS_END);
  const rsrc_t rk = mk_rsrc(kc + (size_t)(b * NH + h) * SK * DH, SK * DH * 2);
  const rsrc_t rv = mk_rsrc(vc + (size_t)(b * NH + h) * SK * DH, SK * DH * 2);
  const rsrc_t rx = mk_rsrc(x, NB * DM * 2);
  const rsrc_t rq = mk_rsrc(wq, DM * NH * DH * 2);
  const rsrc_t ro = mk_rsrc(wo + (size_t)h * DH * DM, DH * DM * 2);
  const u32 kvoff = lane * 16;
  const u32 kvbase = (s * 8 + w) * 8192;
#define KVOFF(i) (kvbase + ((i) >> 3) * 262144 + ((i) & 7) * 1024)

  const u32 epoch = ld32<AUX_SC1>(rws, 0, WS_E);
  u32x4 xv[2], wqv[2];
  xv[0] = ld128<AUX_NT>(rx, lane * 16, 0);
  xv[1] = ld128<AUX_NT>(rx, lane * 16, 1024);
  {
    const u32 qvoff = r * 8192 + c * 16, qbase = (mem * 64 + w * 8) * 8192 + h * 256;
    wqv[0] = ld128<AUX_NT>(rq, qvoff, qbase);
    wqv[1] = ld128<AUX_NT>(rq, qvoff, qbase + 4 * 8192);
  }
  u32x4 K[32], V[32], WO[16];
#pragma unroll
  for (int i = 0; i < KPRE; ++i) K[i] = ld128<AUX_NT>(rk, kvoff, KVOFF(i));
  SB();
  const u32 tag = __builtin_amdgcn_readfirstlane(epoch) + 1u;

  {
    float xn[2][2];
#pragma unroll
    for (int bb = 0; bb < 2; ++bb) {
      float v[8];
#pragma unroll
      for (int i = 0; i < 4; ++i) { v[2 * i] = bf_lo(xv[bb][i]); v[2 * i + 1] = bf_hi(xv[bb][i]); }
      float mb, den;
      ln_stats(v, mb, den);
      const int src = mem * 8 + w;
#pragma unroll
      for (int i = 0; i < 2; ++i) {
        const u32 d0 = __builtin_amdgcn_readlane(xv[bb][2 * i], src), d1 = __builtin_amdgcn_readlane(xv[bb][2 * i + 1], src);
        const u32 d = (r & 2) ? d1 : d0;
        const float xe = (r & 1) ? bf_hi(d) : bf_lo(d);
        xn[bb][i] = ln_apply(xe, mb, den);
      }
    }
    float a[2][8];
#pragma unroll
    for (int e = 0; e < 4; ++e) {
      const float l0 = bf_lo(wqv[0][e]), h0 = bf_hi(wqv[0][e]), l1 = bf_lo(wqv[1][e]), h1 = bf_hi(wqv[1][e]);
#pragma unroll
      for (int bb = 0; bb < 2; ++bb) {
        a[bb][2 * e] = fmaf(xn[bb][1], l1, xn[bb][0] * l0);
        a[bb][2 * e + 1] = fmaf(xn[bb][1], h1, xn[bb][0] * h0);
      }
    }
    float t[8], u[4];
#pragma unroll
    for (int e = 0; e < 8; ++e) t[e] = swap16_add(a[0][e], a[1][e]);
#pragma unroll
    for (int e = 0; e < 4; ++e) u[e] = swap32_add(t[e], t[e + 4]);
    *reinterpret_cast<f32x4*>(&L.qp[w][r & 1][8 * c + 4 * (r >> 1)]) = f32x4{u[0], u[1], u[2], u[3]};
  }
  WG_BARRIER();
  if (w < 2) {
    float q0 = 0.f, q1 = 0.f;
#pragma unroll
    for (int i = 0; i < 8; ++i) { const f32x2 v = *reinterpret_cast<const f32x2*>(&L.qp[i][w][2 * lane]); q0 += v[0]; q1 += v[1]; }
    st_unit(rws, WS_Q + (((h * 8 + mem) * 2 + w) * 64 + lane) * 16, asu(q0), asu(q1), tag);
  }
  STAMP(1);
  SB();

  const u32 qgoff = WS_Q + (((h * 8 + w) * 2 + b) * 64 + lane) * 16;
  u32x4 qg;
#pragma unroll
  for (int i = KPRE; i < 32; ++i) {
    if (i >= WIN) WAITVM_N(WIN - 1);
    K[i] = ld128<AUX_NT>(rk, kvoff, KVOFF(i));
    if (i == QPOS - 1) qg = ld128<AUX_SC1>(rws, qgoff, 0);
    SB();
  }
  constexpr int VPRE = QPOS + WIN - 32 > 0 ? QPOS + WIN - 32 : 0;
#pragma unroll
  for (int i = 0; i < VPRE; ++i) {
    WAITVM_N(WIN - 1);
    V[i] = ld128<AUX_NT>(rv, kvoff, KVOFF(i));
    SB();
  }
  if (!__all(unit_ok(qg, tag))) {
    u32 spins = 0;
    do {
      asm volatile("" ::: "memory");
      qg = ld128<AUX_SC1>(rws, qgoff, 0);
    } while (!__all(unit_ok(qg, tag)) && ++spins < SPIN_CAP);
  }
  STAMP(2);
  WG_BARRIER();
  *reinterpret_cast<f32x2*>(&L.qp[w][0][2 * lane]) = f32x2{asf(qg[0]), asf(qg[2])};
  WG_BARRIER();
  u32x4 q;
  {
    float q0 = 0.f, q1 = 0.f;
#pragma unroll
    for (int i = 0; i < 8; ++i) { const f32x2 v = *reinterpret_cast<const f32x2*>(&L.qp[i][0][2 * lane]); q0 += v[0]; q1 += v[1]; }
    L.qb[w][lane] = pack_bf16(q0, q1);
    LDS_SYNC();
    q = *reinterpret_cast<const u32x4*>(&L.qb[w][4 * c]);
  }
  STAMP(3);
  float sc[2];
  {
    float t1[2], t2[2], t3[2], t4[2];
#pragma unroll
    for (int i = 0; i < 32; ++i) {
      float d = dot2(K[i][0], q[0], 0.f);
      d = dot2(K[i][1], q[1], d); d = dot2(K[i][2], q[2], d); d = dot2(K[i][3], q[3], d);
      t1[i & 1] = d;
      if ((i & 1) == 1) {
        t2[(i >> 1) & 1] = tr_step<XOR1>(t1[0], t1[1], c & 1);
        if ((i & 3) == 3) {
          t3[(i >> 2) & 1] = tr_step<XOR2>(t2[0], t2[1], c & 2);
          if ((i & 7) == 7) {
            t4[(i >> 3) & 1] = tr_step<ROR4>(t3[0], t3[1], c & 4);
            if ((i & 15) == 15) sc[i >> 4] = tr_step<ROR8>(t4[0], t4[1], c & 8);
          }
        }
      }
      if (i >= QPOS && VPRE + (i - QPOS) < WIN) {
        SB();
        V[VPRE + i - QPOS] = ld128<AUX_NT>(rv, kvoff, KVOFF(VPRE + i - QPOS));
        SB();
      }
    }
  }
  constexpr int VISS = (VPRE + 32 - QPOS) < WIN ? (VPRE + 32 - QPOS) : WIN;
  float Mwg, Lwg;
  {
    float s0 = rbf(rbf(sc[0]) * 0.08838834764831845f), s1 = rbf(rbf(sc[1]) * 0.08838834764831845f);
    const float mw = wave_max(fmaxf(s0, s1));
    float p0 = ex2((s0 - mw) * LOG2E), p1 = ex2((s1 - mw) * LOG2E);
    const float lw = wave_sum(p0 + p1);
    if (lane == 0) *reinterpret_cast<f32x2*>(&L.ml[w][0]) = f32x2{mw, lw};
    WG_BARRIER();
    float mm[8], ll[8];
#pragma unroll
    for (int i = 0; i < 4; ++i) { const f32x4 v = *reinterpret_cast<const f32x4*>(&L.ml[2 * i][0]); mm[2 * i] = v[0]; ll[2 * i] = v[1]; mm[2 * i + 1] = v[2]; ll[2 * i + 1] = v[3]; }
    Mwg = fmaxf(fmaxf(fmaxf(mm[0], mm[1]), fmaxf(mm[2], mm[3])), fmaxf(fmaxf(mm[4], mm[5]), fmaxf(mm[6], mm[7])));
    Lwg = 0.f;
#pragma unroll
    for (int i = 0; i < 8; ++i) Lwg = fmaf(ll[i], ex2((mm[i] - Mwg) * LOG2E), Lwg);
    const float f = ex2((mw - Mwg) * LOG2E);
    L.p[w][r][c] = p0 * f; L.p[w][r][16 + c] = p1 * f;
    if (tid == 0) st_unit(rws, WS_ML + ((b * 32 + h) * 4 + s) * 64, asu(Mwg), asu(Lwg), tag);
    LDS_SYNC();
  }
  STAMP(4);
  const bool is_ffn = bid < NFW;
  const int fid = bid;
  const rsrc_t r1 = mk_rsrc(w1, DM * FF * 2);
  const rsrc_t r2 = mk_rsrc(w2, FF * DM * 2);
  const u32 ovoff = r8 * 2048 + c8 * 16 + w * 128;
  const u32 w1voff = is_ffn ? (u32)((64 * w + 4 * (lane >> 2)) * (FF * 2) + fid * 64 + (lane & 3) * 16) : OOB;
  const u32 w2voff = is_ffn ? (u32)((fid * 32 + 2 * r8) * 1024 + w * 128 + c8 * 16) : OOB;
  const u32 mloff = WS_ML + ((b * 32 + h) * 4 + (lane & 3)) * 64;
  u32x4 W1[4], W2[4], mlv;
  float acc[8];
#pragma unroll
  for (int k = 0; k < 8; ++k) acc[k] = 0.f;
  f32x4 pv;
  constexpr int NV2 = 32 - VISS;
  auto pv_step = [&](int i) {
    if ((i & 3) == 0) pv = *reinterpret_cast<const f32x4*>(&L.p[w][r][i]);
    const float p = pv[i & 3];
#pragma unroll
    for (int e = 0; e < 4; ++e) { acc[2 * e] = fmaf(p, bf_lo(V[i][e]), acc[2 * e]); acc[2 * e + 1] = fmaf(p, bf_hi(V[i][e]), acc[2 * e + 1]); }
    asm volatile("" : "+v"(acc[0]), "+v"(acc[1]), "+v"(acc[2]), "+v"(acc[3]), "+v"(acc[4]), "+v"(acc[5]), "+v"(acc[6]), "+v"(acc[7]));
  };
  auto wo_perm = [&](int j) {
#pragma unroll
    for (int e = 0; e < 4; ++e) {
      const u32 A = WO[2 * j][e], Bq = WO[2 * j + 1][e];
      WO[2 * j][e] = __builtin_amdgcn_perm(Bq, A, 0x05040100u);
      WO[2 * j + 1][e] = __builtin_amdgcn_perm(Bq, A, 0x07060302u);
    }
    asm volatile("" : "+v"(WO[2 * j]), "+v"(WO[2 * j + 1]));
  };
  static_assert(VISS == 16 && NV2 == 16, "sequence below assumes 16 V loads issued");
#pragma unroll
  for (int k = 0; k < 16; ++k) {
    if (k < 8) { pv_step(2 * k); } else { if (((k - 8) & 1) == 1) wo_perm((k - 8) >> 1); }
    SB();
    WO[k] = ld128<AUX_NT>(ro, ovoff, ((k >> 1) * 16 + (k & 1)) * 1024);
    SB();
    if (k < 8) { pv_step(2 * k + 1); } else { pv_step(16 + (k - 8)); }
    SB();
    V[16 + k] = ld128<AUX_NT>(rv, kvoff, KVOFF(16 + k));
    if (k == 10) mlv = ld128<AUX_SC1>(rws, mloff, 0);
    SB();
  }
  float oscale;
#pragma unroll
  for (int k = 0; k < 8; ++k) {
    if ((k & 1) == 1) wo_perm((8 + k) >> 1);
    SB();
    if (k < 4) W1[k] = ld128<AUX_NT>(r1, w1voff, k * (FF * 2));
    else W2[k - 4] = ld128<AUX_NT>(r2, w2voff, (((k - 4) >> 1) * 16 + ((k - 4) & 1)) * 1024);
    SB();
    pv_step(24 + k);
    SB();
    if (k == 3) {
      if (!__all(unit_ok(mlv, tag))) {
        u32 spins = 0;
        do {
          asm volatile("" ::: "memory");
          mlv = ld128<AUX_SC1>(rws, mloff, 0);
        } while (!__all(unit_ok(mlv, tag)) && ++spins < SPIN_CAP);
      }
      const float ms = asf(mlv[0]), ls = asf(mlv[2]);
      float M = fmaxf(ms, dpp<XOR1>(ms)); M = fmaxf(M, dpp<XOR2>(M));
      float Ls = ls * ex2((ms - M) * LOG2E);
      Ls += dpp<XOR1>(Ls); Ls += dpp<XOR2>(Ls);
      oscale = ex2((Mwg - M) * LOG2E) / Ls;
      SB();
    }
  }
  STAMP(5);
  {
    float t[4], u[2];
#pragma unroll
    for (int e = 0; e < 4; ++e) t[e] = swap16_add(acc[e], acc[e + 4]);
#pragma unroll
    for (int e = 0; e < 2; ++e) u[e] = swap32_add(t[e], t[e + 2]);
    *reinterpret_cast<f32x2*>(&L.acc[w][8 * c + 4 * (r & 1) + 2 * (r >> 1)]) = f32x2{u[0], u[1]};
  }
  WG_BARRIER();
  u32 op[8];
  {
    float o0 = 0.f, o1 = 0.f;
#pragma unroll
    for (int i = 0; i < 8; ++i) { const f32x2 v = *reinterpret_cast<const f32x2*>(&L.acc[i][2 * lane]); o0 += v[0]; o1 += v[1]; }
    L.qb[w][c8 * 8 + r8] = pack_bf16(o0 * oscale, o1 * oscale);
    LDS_SYNC();
    const u32x4 a = *reinterpret_cast<const u32x4*>(&L.qb[w][r8 * 8]), bq = *reinterpret_cast<const u32x4*>(&L.qb[w][r8 * 8 + 4]);
    op[0] = a[0]; op[1] = a[1]; op[2] = a[2]; op[3] = a[3]; op[4] = bq[0]; op[5] = bq[1]; op[6] = bq[2]; op[7] = bq[3];
  }
  {
    float y[8];
#pragma unroll
    for (int e = 0; e < 8; ++e) y[e] = 0.f;
#pragma unroll
    for (int j = 0; j < 8; ++j)
#pragma unroll
      for (int e = 0; e < 4; ++e) { y[2 * e] = dot2(WO[2 * j][e], op[j], y[2 * e]); y[2 * e + 1] = dot2(WO[2 * j + 1][e], op[j], y[2 * e + 1]); }
    float t[4], u[2];
#pragma unroll
    for (int e = 0; e < 4; ++e) t[e] = tr_step<ROR8>(y[e], y[e + 4], lane & 8);
#pragma unroll
    for (int e = 0; e < 2; ++e) u[e] = swap16_add(t[e], t[e + 2]);
    u[0] = swap32_add(u[0], u[0]); u[1] = swap32_add(u[1], u[1]);
    const u32 unit = w * 32 + c8 * 4 + ((lane >> 3) & 1) * 2 + ((lane >> 4) & 1);
    if (lane < 32) st_unit(rws, WS_Y + ((b * 128 + h * 4 + s) * 256 + unit) * 16, asu(u[0]), asu(u[1]), tag);
  }
  STAMP(6);

  if (bid >= 128) {
    if (w == 0) {
      const int rid = bid - 128, rb = rid >> 6, cg = rid & 63;
      const u32 xpair = ld32<AUX_NT>(rx, (rb * DM + cg * 8 + (lane & 3) * 2) * 2, 0);
      const rsrc_t rb2 = mk_rsrc(b2, DM * 2);
      const u32 b2pair = ld32<AUX_NT>(rb2, (cg * 8 + (lane & 3) * 2) * 2, 0);
      const u32 yoff = WS_Y + ((rb * 128 + (lane >> 2)) * 256 + cg * 4 + (lane & 3)) * 16;
      u32x4 v[8];
#pragma unroll
      for (int n = 0; n < 8; ++n) v[n] = ld128<AUX_SC1>(rws, yoff, n * 16 * 4096);
      u32 spins = 0;
      for (;;) {
        bool ok = true;
#pragma unroll
        for (int n = 0; n < 8; ++n) ok &= unit_ok(v[n], tag);
        if (__all(ok) || ++spins >= SPIN_CAP) break;
        asm volatile("" ::: "memory");
        u32x4 nv[8];
#pragma unroll
        for (int n = 0; n < 8; ++n) nv[n] = ld128<AUX_SC1>(rws, unit_ok(v[n], tag) ? OOB : yoff, n * 16 * 4096);
#pragma unroll
        for (int n = 0; n < 8; ++n) if (!unit_ok(v[n], tag)) v[n] = nv[n];
      }
      STAMP(7);
      float a0 = 0.f, a1 = 0.f;
#pragma unroll
      for (int n = 0; n < 8; ++n) { a0 += asf(v[n][0]); a1 += asf(v[n][2]); }
      a0 += dpp<ROR8>(a0); a1 += dpp<ROR8>(a1);
      a0 += dpp<ROR4>(a0); a1 += dpp<ROR4>(a1);
      a0 = swap16_add(a0, a0); a1 = swap16_add(a1, a1);
      a0 = swap32_add(a0, a0); a1 = swap32_add(a1, a1);
      const float h0 = rbf(bf_lo(xpair) + rbf(a0)), h1 = rbf(bf_hi(xpair) + rbf(a1));
      const u32 hp = pack_bf16(h0, h1);
      const u32 hn = dppu<XOR1>(hp);
      if (lane == 0 || lane == 2) st_unit(rws, WS_H + (rb * 128 + cg * 2 + (lane >> 1)) * 16, hp, hn, tag);
      STAMP(8);
      const int pr = lane >> 2;
      const u32 foff = WS_F + ((pr * 2 + rb) * 256 + cg * 4 + (lane & 3)) * 16;
      u32x4 f[3];
      const u32 foff2 = (pr + 32 < NFW) ? foff : OOB;
      f[0] = ld128<AUX_SC1>(rws, foff, 0); f[1] = ld128<AUX_SC1>(rws, foff, 16 * 8192); f[2] = ld128<AUX_SC1>(rws, foff2, 32 * 8192);
      spins = 0;
      for (;;) {
        const bool ok2 = unit_ok(f[2], tag) || (pr + 32 >= NFW);
        const bool ok = unit_ok(f[0], tag) && unit_ok(f[1], tag) && ok2;
        if (__all(ok) || ++spins >= SPIN_CAP) break;
        asm volatile("" ::: "memory");
        u32x4 n0 = ld128<AUX_SC1>(rws, unit_ok(f[0], tag) ? OOB : foff, 0);
        u32x4 n1 = ld128<AUX_SC1>(rws, unit_ok(f[1], tag) ? OOB : foff, 16 * 8192);
        u32x4 n2 = ld128<AUX_SC1>(rws, ok2 ? OOB : foff, 32 * 8192);
        if (!unit_ok(f[0], tag)) f[0] = n0;
        if (!unit_ok(f[1], tag)) f[1] = n1;
        if (!ok2) f[2] = n2;
      }
      STAMP(9);
      float g0 = (asf(f[0][0]) + asf(f[1][0])) + asf(f[2][0]), g1 = (asf(f[0][2]) + asf(f[1][2])) + asf(f[2][2]);
      g0 += dpp<ROR8>(g0); g1 += dpp<ROR8>(g1);
      g0 += dpp<ROR4>(g0); g1 += dpp<ROR4>(g1);
      g0 = swap16_add(g0, g0); g1 = swap16_add(g1, g1);
      g0 = swap32_add(g0, g0); g1 = swap32_add(g1, g1);
      const float o0 = h0 + rbf(rbf(g0) + bf_lo(b2pair)), o1 = h1 + rbf(rbf(g1) + bf_hi(b2pair));
      if (lane < 4) reinterpret_cast<u32*>(out)[(rb * DM + cg * 8) / 2 + lane] = pack_bf16(o0, o1);
      if (rid == 0 && lane == 0) __builtin_amdgcn_raw_buffer_store_b32(tag, rws, (int)WS_E, 0, AUX_SC1);
      STAMP(10);
    }
  } else if (is_ffn) {
    u32 P1[2][8], P2[2][8];
#pragma unroll
    for (int j = 0; j < 2; ++j)
#pragma unroll
      for (int e = 0; e < 4; ++e) {
        P1[j][2 * e] = __builtin_amdgcn_perm(W1[2 * j + 1][e], W1[2 * j][e], 0x05040100u);
        P1[j][2 * e + 1] = __builtin_amdgcn_perm(W1[2 * j + 1][e], W1[2 * j][e], 0x07060302u);
        P2[j][2 * e] = __builtin_amdgcn_perm(W2[2 * j + 1][e], W2[2 * j][e], 0x05040100u);
        P2[j][2 * e + 1] = __builtin_amdgcn_perm(W2[2 * j + 1][e], W2[2 * j][e], 0x07060302u);
      }
    const rsrc_t rb1 = mk_rsrc(b1, FF * 2);
    const u32 b1raw = __builtin_amdgcn_raw_buffer_load_b16(rb1, (fid * 32 + (lane & 31)) * 2, 0, AUX_NT);
    const u32 hoff = WS_H + lane * 16, hoff2 = WS_H + (16 * w + (lane >> 2)) * 16;
    u32x4 hv[6];
    u32 spins = 0;
    for (;;) {
      asm volatile("" ::: "memory");
      hv[0] = ld128<AUX_SC1>(rws, hoff, 0); hv[1] = ld128<AUX_SC1>(rws, hoff, 1024);
      hv[2] = ld128<AUX_SC1>(rws, hoff, 2048); hv[3] = ld128<AUX_SC1>(rws, hoff, 3072);
      hv[4] = ld128<AUX_SC1>(rws, hoff2, 0); hv[5] = ld128<AUX_SC1>(rws, hoff2, 2048);
      bool ok = true;
#pragma unroll
      for (int n = 0; n < 6; ++n) ok &= unit_ok(hv[n], tag);
      if (__all(ok) || ++spins >= SPIN_CAP) break;
    }
    STAMP(7);
    float zz[2][8];
#pragma unroll
    for (int bb = 0; bb < 2; ++bb) {
      float v[8] = {bf_lo(hv[2 * bb][0]), bf_hi(hv[2 * bb][0]), bf_lo(hv[2 * bb][2]), bf_hi(hv[2 * bb][2]),
                    bf_lo(hv[2 * bb + 1][0]), bf_hi(hv[2 * bb + 1][0]), bf_lo(hv[2 * bb + 1][2]), bf_hi(hv[2 * bb + 1][2])};
      float mb, den;
      ln_stats(v, mb, den);
      const u32 n0 = pack_bf16(ln_apply(bf_lo(hv[4 + bb][0]), mb, den), ln_apply(bf_hi(hv[4 + bb][0]), mb, den));
      const u32 n1 = pack_bf16(ln_apply(bf_lo(hv[4 + bb][2]), mb, den), ln_apply(bf_hi(hv[4 + bb][2]), mb, den));
#pragma unroll
      for (int e = 0; e < 8; ++e) zz[bb][e] = dot2(P1[1][e], n1, dot2(P1[0][e], n0, 0.f));
    }
    {
      float t[8], u[4], v2[2];
#pragma unroll
      for (int bb = 0; bb < 2; ++bb)
#pragma unroll
        for (int e = 0; e < 4; ++e) t[bb * 4 + e] = tr_step<ROR4>(zz[bb][e], zz[bb][e + 4], lane & 4);
#pragma unroll
      for (int bb = 0; bb < 2; ++bb)
#pragma unroll
        for (int e = 0; e < 2; ++e) u[bb * 2 + e] = tr_step<ROR8>(t[bb * 4 + e], t[bb * 4 + e + 2], lane & 8);
      v2[0] = swap16_add(u[0], u[1]); v2[1] = swap16_add(u[2], u[3]);
      const float z = swap32_add(v2[0], v2[1]);
      const int col = 8 * (lane & 3) + ((lane >> 2) & 1) * 4 + ((lane >> 3) & 1) * 2 + ((lane >> 4) & 1);
      L.z[w][(lane >> 5) * 32 + col] = z;
    }
    WG_BARRIER();
    u32 gp[2][2];
    {
      float z = 0.f;
#pragma unroll
      for (int i = 0; i < 8; ++i) z += L.z[i][lane];
      z = rbf(rbf(z) + bf_lo(b1raw));
      const float uu = 0.7978845608028654f * (z + 0.044715f * z * z * z);
      const float th = 1.f - 2.f / (1.f + ex2(2.f * LOG2E * uu));
      const float g = 0.5f * z * (1.f + th);
      const float gn = dpp<XOR1>(g);
      if ((lane & 1) == 0) L.g[w][lane >> 1] = pack_bf16(g, gn);
      LDS_SYNC();
#pragma unroll
      for (int bb = 0; bb < 2; ++bb) { gp[bb][0] = L.g[w][bb * 16 + r8]; gp[bb][1] = L.g[w][bb * 16 + 8 + r8]; }
    }
    {
      float y[2][8];
#pragma unroll
      for (int bb = 0; bb < 2; ++bb)
#pragma unroll
        for (int e = 0; e < 8; ++e) y[bb][e] = dot2(P2[1][e], gp[bb][1], dot2(P2[0][e], gp[bb][0], 0.f));
      float t[8], u[4], v2[2];
#pragma unroll
      for (int bb = 0; bb < 2; ++bb)
#pragma unroll
        for (int e = 0; e < 4; ++e) t[bb * 4 + e] = tr_step<ROR8>(y[bb][e], y[bb][e + 4], lane & 8);
#pragma unroll
      for (int bb = 0; bb < 2; ++bb)
#pragma unroll
        for (int e = 0; e < 2; ++e) u[bb * 2 + e] = swap16_add(t[bb * 4 + e], t[bb * 4 + e + 2]);
      v2[0] = swap32_add(u[0], u[2]); v2[1] = swap32_add(u[1], u[3]);
      const u32 unit = w * 32 + c8 * 4 + ((lane >> 3) & 1) * 2 + ((lane >> 4) & 1);
      st_unit(rws, WS_F + ((fid * 2 + (lane >> 5)) * 256 + unit) * 16, asu(v2[0]), asu(v2[1]), tag);
    }
    STAMP(8);
  }
}

extern "C" void kernel_launch(void* const* d_in, const int* in_sizes, int n_in, void* d_out, int out_size, void* d_ws,
                              size_t ws_size, hipStream_t stream) {
  fused_block<<<256, 512, 0, stream>>>((const u16*)d_in[0], (const u16*)d_in[1], (const u16*)d_in[2], (const u16*)d_in[3],
                                       (const u16*)d_in[4], (const u16*)d_in[6], (const u16*)d_in[7], (const u16*)d_in[8],
                                       (const u16*)d_in[9], (u16*)d_out, (unsigned char*)d_ws);
}
```

```cpp
#include <hip/hip_runtime.h>
#include <stdint.h>

typedef unsigned short u16;
typedef unsigned int u32;
typedef u32 u32x4 __attribute__((ext_vector_type(4)));
typedef u32 u32x2 __attribute__((ext_vector_type(2)));
typedef float f32x4 __attribute__((ext_vector_type(4)));
typedef float f32x2 __attribute__((ext_vector_type(2)));
typedef __bf16 bf16x2 __attribute__((ext_vector_type(2)));
typedef __amdgpu_buffer_rsrc_t rsrc_t;

#define WIN 16
#define KPRE 12
#define QPOS 20
#define STAMPS 0

constexpr int NB = 2, DM = 512, NH = 32, SK = 4096, DH = 128, FF = 1376;
constexpr int NFW = 43;
#define KVAUX 2
#define WAUX 2
#define HLD 16
#define HST 16
constexpr int AUX_NT = WAUX, AUX_SC1 = HLD, AUX_KV = KVAUX, AUX_ST = HST;
constexpr u32 OOB = 0x7ffffff0u;
constexpr u32 WS_E = 0;
constexpr u32 WS_Q = 4096;
constexpr u32 WS_ML = WS_Q + 32 * 8 * 2 * 64 * 16;
constexpr u32 WS_H = WS_ML + 256 * 64;
constexpr u32 WS_Y = WS_H + 4096;
constexpr u32 WS_F = WS_Y + 2 * 128 * 256 * 16;
constexpr u32 WS_END = WS_F + NFW * 2 * 256 * 16;
constexpr u32 WS_TS = 8u << 20;
constexpr u32 SPIN_CAP = 1u << 20;

#define SB() __builtin_amdgcn_sched_barrier(0)
#define STR2(x) #x
#define STR(x) STR2(x)
#define WAITVM_N(n) asm volatile("s_waitcnt vmcnt(%0)" ::"n"(n) : "memory")
#define LDS_SYNC() do { asm volatile("s_waitcnt lgkmcnt(0)" ::: "memory"); } while (0)
#define WG_BARRIER() do { asm volatile("s_waitcnt lgkmcnt(0)\n\ts_barrier" ::: "memory"); } while (0)

__device__ __forceinline__ float asf(u32 v) { return __builtin_bit_cast(float, v); }
__device__ __forceinline__ u32 asu(float v) { return __builtin_bit_cast(u32, v); }
__device__ __forceinline__ float bf_lo(u32 v) { return asf(v << 16); }
__device__ __forceinline__ float bf_hi(u32 v) { return asf(v & 0xffff0000u); }
__device__ __forceinline__ u32 pack_bf16(float lo, float hi) {
  return __builtin_bit_cast(u32, __builtin_convertvector(f32x2{lo, hi}, bf16x2));
}
__device__ __forceinline__ float rbf(float x) { return bf_lo(pack_bf16(x, x)); }
__device__ __forceinline__ float dot2(u32 a, u32 b, float c) {
  return __builtin_amdgcn_fdot2_f32_bf16(__builtin_bit_cast(bf16x2, a), __builtin_bit_cast(bf16x2, b), c, false);
}
template <int CTRL> __device__ __forceinline__ float dpp(float x) {
  return asf((u32)__builtin_amdgcn_mov_dpp((int)asu(x), CTRL, 0xf, 0xf, true));
}
template <int CTRL> __device__ __forceinline__ u32 dppu(u32 x) { return (u32)__builtin_amdgcn_mov_dpp((int)x, CTRL, 0xf, 0xf, true); }
constexpr int XOR1 = 0xB1, XOR2 = 0x4E, HMIR = 0x141, MIR = 0x140, ROR4 = 0x124, ROR8 = 0x128;
__device__ __forceinline__ float swap16_add(float a, float b) {
  auto r = __builtin_amdgcn_permlane16_swap(asu(a), asu(b), false, false);
  return asf(r[0]) + asf(r[1]);
}
__device__ __forceinline__ float swap32_add(float a, float b) {
  auto r = __builtin_amdgcn_permlane32_swap(asu(a), asu(b), false, false);
  return asf(r[0]) + asf(r[1]);
}
__device__ __forceinline__ float swap16_max(float a) {
  auto r = __builtin_amdgcn_permlane16_swap(asu(a), asu(a), false, false);
  return fmaxf(asf(r[0]), asf(r[1]));
}
__device__ __forceinline__ float swap32_max(float a) {
  auto r = __builtin_amdgcn_permlane32_swap(asu(a), asu(a), false, false);
  return fmaxf(asf(r[0]), asf(r[1]));
}
__device__ __forceinline__ float wave_sum(float x) {
  x += dpp<XOR1>(x); x += dpp<XOR2>(x); x += dpp<HMIR>(x); x += dpp<MIR>(x);
  x = swap16_add(x, x); x = swap32_add(x, x);
  return x;
}
__device__ __forceinline__ float wave_max(float x) {
  x = fmaxf(x, dpp<XOR1>(x)); x = fmaxf(x, dpp<XOR2>(x)); x = fmaxf(x, dpp<HMIR>(x)); x = fmaxf(x, dpp<MIR>(x));
  x = swap16_max(x); x = swap32_max(x);
  return x;
}
template <int CTRL> __device__ __forceinline__ float tr_step(float a, float b, bool sel) {
  const float keep = sel ? b : a, give = sel ? a : b;
  return keep + dpp<CTRL>(give);
}
__device__ __forceinline__ rsrc_t mk_rsrc(const void* p, u32 bytes) {
  return __builtin_amdgcn_make_buffer_rsrc(const_cast<void*>(p), (short)0, (int)bytes, 0x00020000);
}
template <int AUX> __device__ __forceinline__ u32x4 ld128(rsrc_t r, u32 voff, u32 soff) {
  return __builtin_amdgcn_raw_buffer_load_b128(r, (int)voff, (int)soff, AUX);
}
template <int AUX> __device__ __forceinline__ u32 ld32(rsrc_t r, u32 voff, u32 soff) {
  return __builtin_amdgcn_raw_buffer_load_b32(r, (int)voff, (int)soff, AUX);
}
__device__ __forceinline__ void st_unit(rsrc_t r, u32 off, u32 a, u32 b, u32 tag) {
  __builtin_amdgcn_raw_buffer_store_b128(u32x4{a, tag, b, tag}, r, (int)off, 0, AUX_ST);
}
__device__ __forceinline__ bool unit_ok(u32x4 v, u32 tag) { return v[1] == tag && v[3] == tag; }
__device__ __forceinline__ float ex2(float x) { return __builtin_amdgcn_exp2f(x); }
constexpr float LOG2E = 1.4426950408889634f;

__device__ __forceinline__ void ln_stats(const float (&v)[8], float& mb, float& den) {
  float s = ((v[0] + v[1]) + (v[2] + v[3])) + ((v[4] + v[5]) + (v[6] + v[7]));
  const float mean = wave_sum(s) * (1.f / DM);
  float q = 0.f;
#pragma unroll
  for (int i = 0; i < 8; ++i) { const float d = v[i] - mean; q = fmaf(d, d, q); }
  const float var = wave_sum(q) * (1.f / DM);
  mb = rbf(mean);
  den = rbf(sqrtf(rbf(rbf(var) + 1e-5f)));
}
__device__ __forceinline__ float ln_apply(float x, float mb, float den) { return rbf(rbf(x - mb) / den); }

struct Lds {
  float qp[8][2][128];
  u32 qb[8][64];
  float p[8][4][32];
  float ml[8][2];
  float acc[8][128];
  float z[8][64];
  u32 g[8][32];
};

#define STAMP(k) do { } while (0)

__global__ void __launch_bounds__(512, 2)
fused_block(const u16* __restrict__ x, const u16* __restrict__ kc, const u16* __restrict__ vc, const u16* __restrict__ wq,
            const u16* __restrict__ wo, const u16* __restrict__ w1, const u16* __restrict__ b1, const u16* __restrict__ w2,
            const u16* __restrict__ b2, u16* __restrict__ out, unsigned char* __restrict__ ws) {
  __shared__ __attribute__((aligned(16))) Lds L;
  const int tid = threadIdx.x, lane = tid & 63, w = __builtin_amdgcn_readfirstlane(tid >> 6);
  const int r = lane >> 4, c = lane & 15, r8 = lane >> 3, c8 = lane & 7;
  const int bid = blockIdx.x, xcd = bid & 7, jj = bid >> 3;
  const int h = xcd * 4 + (jj >> 3), mem = jj & 7, b = mem >> 2, s = mem & 3;
  STAMP(0);
  const rsrc_t rws = mk_rsrc(ws, WS_END);
  const rsrc_t rk = mk_rsrc(kc + (size_t)(b * NH + h) * SK * DH, SK * DH * 2);
  const rsrc_t rv = mk_rsrc(vc + (size_t)(b * NH + h) * SK * DH, SK * DH * 2);
  const rsrc_t rx = mk_rsrc(x, NB * DM * 2);
  const rsrc_t rq = mk_rsrc(wq, DM * NH * DH * 2);
  const rsrc_t ro = mk_rsrc(wo + (size_t)h * DH * DM, DH * DM * 2);
  const u32 kvoff = lane * 16;
  const u32 kvbase = (s * 8 + w) * 8192;
#define KVOFF(i) (kvbase + ((i) >> 3) * 262144 + ((i) & 7) * 1024)

  const u32 epoch = ld32<AUX_SC1>(rws, 0, WS_E);
  u32x4 xv[2], wqv[2];
  xv[0] = ld128<AUX_NT>(rx, lane * 16, 0);
  xv[1] = ld128<AUX_NT>(rx, lane * 16, 1024);
  {
    const u32 qvoff = r * 8192 + c * 16, qbase = (mem * 64 + w * 8) * 8192 + h * 256;
    wqv[0] = ld128<AUX_NT>(rq, qvoff, qbase);
    wqv[1] = ld128<AUX_NT>(rq, qvoff, qbase + 4 * 8192);
  }
  u32x4 K[32], V[32], WO[16];
#pragma unroll
  for (int i = 0; i < KPRE; ++i) K[i] = ld128<AUX_KV>(rk, kvoff, KVOFF(i));
  SB();
  const u32 tag = __builtin_amdgcn_readfirstlane(epoch) + 1u;

  {
    float xn[2][2];
#pragma unroll
    for (int bb = 0; bb < 2; ++bb) {
      float v[8];
#pragma unroll
      for (int i = 0; i < 4; ++i) { v[2 * i] = bf_lo(xv[bb][i]); v[2 * i + 1] = bf_hi(xv[bb][i]); }
      float mb, den;
      ln_stats(v, mb, den);
      const int src = mem * 8 + w;
#pragma unroll
      for (int i = 0; i < 2; ++i) {
        const u32 d0 = __builtin_amdgcn_readlane(xv[bb][2 * i], src), d1 = __builtin_amdgcn_readlane(xv[bb][2 * i + 1], src);
        const u32 d = (r & 2) ? d1 : d0;
        const float xe = (r & 1) ? bf_hi(d) : bf_lo(d);
        xn[bb][i] = ln_apply(xe, mb, den);
      }
    }
    float a[2][8];
#pragma unroll
    for (int e = 0; e < 4; ++e) {
      const float l0 = bf_lo(wqv[0][e]), h0 = bf_hi(wqv[0][e]), l1 = bf_lo(wqv[1][e]), h1 = bf_hi(wqv[1][e]);
#pragma unroll
      for (int bb = 0; bb < 2; ++bb) {
        a[bb][2 * e] = fmaf(xn[bb][1], l1, xn[bb][0] * l0);
        a[bb][2 * e + 1] = fmaf(xn[bb][1], h1, xn[bb][0] * h0);
      }
    }
    float t[8], u[4];
#pragma unroll
    for (int e = 0; e < 8; ++e) t[e] = swap16_add(a[0][e], a[1][e]);
#pragma unroll
    for (int e = 0; e < 4; ++e) u[e] = swap32_add(t[e], t[e + 4]);
    *reinterpret_cast<f32x4*>(&L.qp[w][r & 1][8 * c + 4 * (r >> 1)]) = f32x4{u[0], u[1], u[2], u[3]};
  }
  WG_BARRIER();
  if (w < 2) {
    float q0 = 0.f, q1 = 0.f;
#pragma unroll
    for (int i = 0; i < 8; ++i) { const f32x2 v = *reinterpret_cast<const f32x2*>(&L.qp[i][w][2 * lane]); q0 += v[0]; q1 += v[1]; }
    st_unit(rws, WS_Q + (((h * 8 + mem) * 2 + w) * 64 + lane) * 16, asu(q0), asu(q1), tag);
  }
  STAMP(1);
  SB();

  const u32 qgoff = WS_Q + (((h * 8 + w) * 2 + b) * 64 + lane) * 16;
  u32x4 qg;
#pragma unroll
  for (int i = KPRE; i < 32; ++i) {
    if (i >= WIN) WAITVM_N(WIN - 1);
    K[i] = ld128<AUX_KV>(rk, kvoff, KVOFF(i));
    if (i == QPOS - 1) qg = ld128<AUX_SC1>(rws, qgoff, 0);
    SB();
  }
  constexpr int VPRE = QPOS + WIN - 32 > 0 ? QPOS + WIN - 32 : 0;
#pragma unroll
  for (int i = 0; i < VPRE; ++i) {
    WAITVM_N(WIN - 1);
    V[i] = ld128<AUX_KV>(rv, kvoff, KVOFF(i));
    SB();
  }
  if (!__all(unit_ok(qg, tag))) {
    u32 spins = 0;
    do {
      asm volatile("" ::: "memory");
      qg = ld128<AUX_SC1>(rws, qgoff, 0);
    } while (!__all(unit_ok(qg, tag)) && ++spins < SPIN_CAP);
  }
  STAMP(2);
  WG_BARRIER();
  *reinterpret_cast<f32x2*>(&L.qp[w][0][2 * lane]) = f32x2{asf(qg[0]), asf(qg[2])};
  WG_BARRIER();
  u32x4 q;
  {
    float q0 = 0.f, q1 = 0.f;
#pragma unroll
    for (int i = 0; i < 8; ++i) { const f32x2 v = *reinterpret_cast<const f32x2*>(&L.qp[i][0][2 * lane]); q0 += v[0]; q1 += v[1]; }
    L.qb[w][lane] = pack_bf16(q0, q1);
    LDS_SYNC();
    q = *reinterpret_cast<const u32x4*>(&L.qb[w][4 * c]);
  }
  STAMP(3);
  float sc[2];
  {
    float t1[2], t2[2], t3[2], t4[2];
#pragma unroll
    for (int i = 0; i < 32; ++i) {
      float d = dot2(K[i][0], q[0], 0.f);
      d = dot2(K[i][1], q[1], d); d = dot2(K[i][2], q[2], d); d = dot2(K[i][3], q[3], d);
      t1[i & 1] = d;
      if ((i & 1) == 1) {
        t2[(i >> 1) & 1] = tr_step<XOR1>(t1[0], t1[1], c & 1);
        if ((i & 3) == 3) {
          t3[(i >> 2) & 1] = tr_step<XOR2>(t2[0], t2[1], c & 2);
          if ((i & 7) == 7) {
            t4[(i >> 3) & 1] = tr_step<ROR4>(t3[0], t3[1], c & 4);
            if ((i & 15) == 15) sc[i >> 4] = tr_step<ROR8>(t4[0], t4[1], c & 8);
          }
        }
      }
      if (i >= QPOS && VPRE + (i - QPOS) < WIN) {
        SB();
        V[VPRE + i - QPOS] = ld128<AUX_KV>(rv, kvoff, KVOFF(VPRE + i - QPOS));
        SB();
      }
    }
  }
  constexpr int VISS = (VPRE + 32 - QPOS) < WIN ? (VPRE + 32 - QPOS) : WIN;
  float Mwg, Lwg;
  {
    float s0 = rbf(rbf(sc[0]) * 0.08838834764831845f), s1 = rbf(rbf(sc[1]) * 0.08838834764831845f);
    const float mw = wave_max(fmaxf(s0, s1));
    float p0 = ex2((s0 - mw) * LOG2E), p1 = ex2((s1 - mw) * LOG2E);
    const float lw = wave_sum(p0 + p1);
    if (lane == 0) *reinterpret_cast<f32x2*>(&L.ml[w][0]) = f32x2{mw, lw};
    WG_BARRIER();
    float mm[8], ll[8];
#pragma unroll
    for (int i = 0; i < 4; ++i) { const f32x4 v = *reinterpret_cast<const f32x4*>(&L.ml[2 * i][0]); mm[2 * i] = v[0]; ll[2 * i] = v[1]; mm[2 * i + 1] = v[2]; ll[2 * i + 1] = v[3]; }
    Mwg = fmaxf(fmaxf(fmaxf(mm[0], mm[1]), fmaxf(mm[2], mm[3])), fmaxf(fmaxf(mm[4], mm[5]), fmaxf(mm[6], mm[7])));
    Lwg = 0.f;
#pragma unroll
    for (int i = 0; i < 8; ++i) Lwg = fmaf(ll[i], ex2((mm[i] - Mwg) * LOG2E), Lwg);
    const float f = ex2((mw - Mwg) * LOG2E);
    L.p[w][r][c] = p0 * f; L.p[w][r][16 + c] = p1 * f;
    if (tid == 0) st_unit(rws, WS_ML + ((b * 32 + h) * 4 + s) * 64, asu(Mwg), asu(Lwg), tag);
    LDS_SYNC();
  }
  STAMP(4);
  const bool is_ffn = bid < NFW;
  const int fid = bid;
  const rsrc_t r1 = mk_rsrc(w1, DM * FF * 2);
  const rsrc_t r2 = mk_rsrc(w2, FF * DM * 2);
  const u32 ovoff = r8 * 2048 + c8 * 16 + w * 128;
  const u32 w1voff = is_ffn ? (u32)((64 * w + 4 * (lane >> 2)) * (FF * 2) + fid * 64 + (lane & 3) * 16) : OOB;
  const u32 w2voff = is_ffn ? (u32)((fid * 32 + 2 * r8) * 1024 + w * 128 + c8 * 16) : OOB;
  const u32 mloff = WS_ML + ((b * 32 + h) * 4 + (lane & 3)) * 64;
  u32x4 W1[4], W2[4], mlv;
  float acc[8];
#pragma unroll
  for (int k = 0; k < 8; ++k) acc[k] = 0.f;
  f32x4 pv;
  constexpr int NV2 = 32 - VISS;
  auto pv_step = [&](int i) {
    if ((i & 3) == 0) pv = *reinterpret_cast<const f32x4*>(&L.p[w][r][i]);
    const float p = pv[i & 3];
#pragma unroll
    for (int e = 0; e < 4; ++e) { acc[2 * e] = fmaf(p, bf_lo(V[i][e]), acc[2 * e]); acc[2 * e + 1] = fmaf(p, bf_hi(V[i][e]), acc[2 * e + 1]); }
    asm volatile("" : "+v"(acc[0]), "+v"(acc[1]), "+v"(acc[2]), "+v"(acc[3]), "+v"(acc[4]), "+v"(acc[5]), "+v"(acc[6]), "+v"(acc[7]));
  };
  auto wo_perm = [&](int j) {
#pragma unroll
    for (int e = 0; e < 4; ++e) {
      const u32 A = WO[2 * j][e], Bq = WO[2 * j + 1][e];
      WO[2 * j][e] = __builtin_amdgcn_perm(Bq, A, 0x05040100u);
      WO[2 * j + 1][e] = __builtin_amdgcn_perm(Bq, A, 0x07060302u);
    }
    asm volatile("" : "+v"(WO[2 * j]), "+v"(WO[2 * j + 1]));
  };
  static_assert(VISS == 16 && NV2 == 16, "sequence below assumes 16 V loads issued");
#pragma unroll
  for (int k = 0; k < 16; ++k) {
    if (k < 8) { pv_step(2 * k); } else { if (((k - 8) & 1) == 1) wo_perm((k - 8) >> 1); }
    SB();
    WO[k] = ld128<AUX_NT>(ro, ovoff, ((k >> 1) * 16 + (k & 1)) * 1024);
    SB();
    if (k < 8) { pv_step(2 * k + 1); } else { pv_step(16 + (k - 8)); }
    SB();
    V[16 + k] = ld128<AUX_KV>(rv, kvoff, KVOFF(16 + k));
    if (k == 10) mlv = ld128<AUX_SC1>(rws, mloff, 0);
    SB();
  }
  float oscale;
#pragma unroll
  for (int k = 0; k < 8; ++k) {
    if ((k & 1) == 1) wo_perm((8 + k) >> 1);
    SB();
    if (k < 4) W1[k] = ld128<AUX_NT>(r1, w1voff, k * (FF * 2));
    else W2[k - 4] = ld128<AUX_NT>(r2, w2voff, (((k - 4) >> 1) * 16 + ((k - 4) & 1)) * 1024);
    SB();
    pv_step(24 + k);
    SB();
    if (k == 3) {
      if (!__all(unit_ok(mlv, tag))) {
        u32 spins = 0;
        do {
          asm volatile("" ::: "memory");
          mlv = ld128<AUX_SC1>(rws, mloff, 0);
        } while (!__all(unit_ok(mlv, tag)) && ++spins < SPIN_CAP);
      }
      const float ms = asf(mlv[0]), ls = asf(mlv[2]);
      float M = fmaxf(ms, dpp<XOR1>(ms)); M = fmaxf(M, dpp<XOR2>(M));
      float Ls = ls * ex2((ms - M) * LOG2E);
      Ls += dpp<XOR1>(Ls); Ls += dpp<XOR2>(Ls);
      oscale = ex2((Mwg - M) * LOG2E) / Ls;
      SB();
    }
  }
  STAMP(5);
  {
    float t[4], u[2];
#pragma unroll
    for (int e = 0; e < 4; ++e) t[e] = swap16_add(acc[e], acc[e + 4]);
#pragma unroll
    for (int e = 0; e < 2; ++e) u[e] = swap32_add(t[e], t[e + 2]);
    *reinterpret_cast<f32x2*>(&L.acc[w][8 * c + 4 * (r & 1) + 2 * (r >> 1)]) = f32x2{u[0], u[1]};
  }
  WG_BARRIER();
  u32 op[8];
  {
    float o0 = 0.f, o1 = 0.f;
#pragma unroll
    for (int i = 0; i < 8; ++i) { const f32x2 v = *reinterpret_cast<const f32x2*>(&L.acc[i][2 * lane]); o0 += v[0]; o1 += v[1]; }
    L.qb[w][c8 * 8 + r8] = pack_bf16(o0 * oscale, o1 * oscale);
    LDS_SYNC();
    const u32x4 a = *reinterpret_cast<const u32x4*>(&L.qb[w][r8 * 8]), bq = *reinterpret_cast<const u32x4*>(&L.qb[w][r8 * 8 + 4]);
    op[0] = a[0]; op[1] = a[1]; op[2] = a[2]; op[3] = a[3]; op[4] = bq[0]; op[5] = bq[1]; op[6] = bq[2]; op[7] = bq[3];
  }
  {
    float y[8];
#pragma unroll
    for (int e = 0; e < 8; ++e) y[e] = 0.f;
#pragma unroll
    for (int j = 0; j < 8; ++j)
#pragma unroll
      for (int e = 0; e < 4; ++e) { y[2 * e] = dot2(WO[2 * j][e], op[j], y[2 * e]); y[2 * e + 1] = dot2(WO[2 * j + 1][e], op[j], y[2 * e + 1]); }
    float t[4], u[2];
#pragma unroll
    for (int e = 0; e < 4; ++e) t[e] = tr_step<ROR8>(y[e], y[e + 4], lane & 8);
#pragma unroll
    for (int e = 0; e < 2; ++e) u[e] = swap16_add(t[e], t[e + 2]);
    u[0] = swap32_add(u[0], u[0]); u[1] = swap32_add(u[1], u[1]);
    const u32 unit = w * 32 + c8 * 4 + ((lane >> 3) & 1) * 2 + ((lane >> 4) & 1);
    if (lane < 32) st_unit(rws, WS_Y + ((b * 128 + h * 4 + s) * 256 + unit) * 16, asu(u[0]), asu(u[1]), tag);
  }
  STAMP(6);

  if (bid >= 128) {
    if (w == 0) {
      const int rid = bid - 128, rb = rid >> 6, cg = rid & 63;
      const u32 xpair = ld32<AUX_NT>(rx, (rb * DM + cg * 8 + (lane & 3) * 2) * 2, 0);
      const rsrc_t rb2 = mk_rsrc(b2, DM * 2);
      const u32 b2pair = ld32<AUX_NT>(rb2, (cg * 8 + (lane & 3) * 2) * 2, 0);
      const u32 yoff = WS_Y + ((rb * 128 + (lane >> 2)) * 256 + cg * 4 + (lane & 3)) * 16;
      u32x4 v[8];
#pragma unroll
      for (int n = 0; n < 8; ++n) v[n] = ld128<AUX_SC1>(rws, yoff, n * 16 * 4096);
      u32 spins = 0;
      for (;;) {
        bool ok = true;
#pragma unroll
        for (int n = 0; n < 8; ++n) ok &= unit_ok(v[n], tag);
        if (__all(ok) || ++spins >= SPIN_CAP) break;
        asm volatile("" ::: "memory");
        u32x4 nv[8];
#pragma unroll
        for (int n = 0; n < 8; ++n) nv[n] = ld128<AUX_SC1>(rws, unit_ok(v[n], tag) ? OOB : yoff, n * 16 * 4096);
#pragma unroll
        for (int n = 0; n < 8; ++n) if (!unit_ok(v[n], tag)) v[n] = nv[n];
      }
      STAMP(7);
      float a0 = 0.f, a1 = 0.f;
#pragma unroll
      for (int n = 0; n < 8; ++n) { a0 += asf(v[n][0]); a1 += asf(v[n][2]); }
      a0 += dpp<ROR8>(a0); a1 += dpp<ROR8>(a1);
      a0 += dpp<ROR4>(a0); a1 += dpp<ROR4>(a1);
      a0 = swap16_add(a0, a0); a1 = swap16_add(a1, a1);
      a0 = swap32_add(a0, a0); a1 = swap32_add(a1, a1);
      const float h0 = rbf(bf_lo(xpair) + rbf(a0)), h1 = rbf(bf_hi(xpair) + rbf(a1));
      const u32 hp = pack_bf16(h0, h1);
      const u32 hn = dppu<XOR1>(hp);
      if (lane == 0 || lane == 2) st_unit(rws, WS_H + (rb * 128 + cg * 2 + (lane >> 1)) * 16, hp, hn, tag);
      STAMP(8);
      const int pr = lane >> 2;
      const u32 foff = WS_F + ((pr * 2 + rb) * 256 + cg * 4 + (lane & 3)) * 16;
      u32x4 f[3];
      const u32 foff2 = (pr + 32 < NFW) ? foff : OOB;
      f[0] = ld128<AUX_SC1>(rws, foff, 0); f[1] = ld128<AUX_SC1>(rws, foff, 16 * 8192); f[2] = ld128<AUX_SC1>(rws, foff2, 32 * 8192);
      spins = 0;
      for (;;) {
        const bool ok2 = unit_ok(f[2], tag) || (pr + 32 >= NFW);
        const bool ok = unit_ok(f[0], tag) && unit_ok(f[1], tag) && ok2;
        if (__all(ok) || ++spins >= SPIN_CAP) break;
        asm volatile("" ::: "memory");
        u32x4 n0 = ld128<AUX_SC1>(rws, unit_ok(f[0], tag) ? OOB : foff, 0);
        u32x4 n1 = ld128<AUX_SC1>(rws, unit_ok(f[1], tag) ? OOB : foff, 16 * 8192);
        u32x4 n2 = ld128<AUX_SC1>(rws, ok2 ? OOB : foff, 32 * 8192);
        if (!unit_ok(f[0], tag)) f[0] = n0;
        if (!unit_ok(f[1], tag)) f[1] = n1;
        if (!ok2) f[2] = n2;
      }
      STAMP(9);
      float g0 = (asf(f[0][0]) + asf(f[1][0])) + asf(f[2][0]), g1 = (asf(f[0][2]) + asf(f[1][2])) + asf(f[2][2]);
      g0 += dpp<ROR8>(g0); g1 += dpp<ROR8>(g1);
      g0 += dpp<ROR4>(g0); g1 += dpp<ROR4>(g1);
      g0 = swap16_add(g0, g0); g1 = swap16_add(g1, g1);
      g0 = swap32_add(g0, g0); g1 = swap32_add(g1, g1);
      const float o0 = h0 + rbf(rbf(g0) + bf_lo(b2pair)), o1 = h1 + rbf(rbf(g1) + bf_hi(b2pair));
      if (lane < 4) reinterpret_cast<u32*>(out)[(rb * DM + cg * 8) / 2 + lane] = pack_bf16(o0, o1);
      if (rid == 0 && lane == 0) __builtin_amdgcn_raw_buffer_store_b32(tag, rws, (int)WS_E, 0, AUX_ST);
      STAMP(10);
    }
  } else if (is_ffn) {
    u32 P1[2][8], P2[2][8];
#pragma unroll
    for (int j = 0; j < 2; ++j)
#pragma unroll
      for (int e = 0; e < 4; ++e) {
        P1[j][2 * e] = __builtin_amdgcn_perm(W1[2 * j + 1][e], W1[2 * j][e], 0x05040100u);
        P1[j][2 * e + 1] = __builtin_amdgcn_perm(W1[2 * j + 1][e], W1[2 * j][e], 0x07060302u);
        P2[j][2 * e] = __builtin_amdgcn_perm(W2[2 * j + 1][e], W2[2 * j][e], 0x05040100u);
        P2[j][2 * e + 1] = __builtin_amdgcn_perm(W2[2 * j + 1][e], W2[2 * j][e], 0x07060302u);
      }
    const rsrc_t rb1 = mk_rsrc(b1, FF * 2);
    const u32 b1raw = __builtin_amdgcn_raw_buffer_load_b16(rb1, (fid * 32 + (lane & 31)) * 2, 0, AUX_NT);
    const u32 hoff = WS_H + lane * 16, hoff2 = WS_H + (16 * w + (lane >> 2)) * 16;
    u32x4 hv[6];
    u32 spins = 0;
    for (;;) {
      asm volatile("" ::: "memory");
      hv[0] = ld128<AUX_SC1>(rws, hoff, 0); hv[1] = ld128<AUX_SC1>(rws, hoff, 1024);
      hv[2] = ld128<AUX_SC1>(rws, hoff, 2048); hv[3] = ld128<AUX_SC1>(rws, hoff, 3072);
      hv[4] = ld128<AUX_SC1>(rws, hoff2, 0); hv[5] = ld128<AUX_SC1>(rws, hoff2, 2048);
      bool ok = true;
#pragma unroll
      for (int n = 0; n < 6; ++n) ok &= unit_ok(hv[n], tag);
      if (__all(ok) || ++spins >= SPIN_CAP) break;
    }
    STAMP(7);
    float zz[2][8];
#pragma unroll
    for (int bb = 0; bb < 2; ++bb) {
      float v[8] = {bf_lo(hv[2 * bb][0]), bf_hi(hv[2 * bb][0]), bf_lo(hv[2 * bb][2]), bf_hi(hv[2 * bb][2]),
                    bf_lo(hv[2 * bb + 1][0]), bf_hi(hv[2 * bb + 1][0]), bf_lo(hv[2 * bb + 1][2]), bf_hi(hv[2 * bb + 1][2])};
      float mb, den;
      ln_stats(v, mb, den);
      const u32 n0 = pack_bf16(ln_apply(bf_lo(hv[4 + bb][0]), mb, den), ln_apply(bf_hi(hv[4 + bb][0]), mb, den));
      const u32 n1 = pack_bf16(ln_apply(bf_lo(hv[4 + bb][2]), mb, den), ln_apply(bf_hi(hv[4 + bb][2]), mb, den));
#pragma unroll
      for (int e = 0; e < 8; ++e) zz[bb][e] = dot2(P1[1][e], n1, dot2(P1[0][e], n0, 0.f));
    }
    {
      float t[8], u[4], v2[2];
#pragma unroll
      for (int bb = 0; bb < 2; ++bb)
#pragma unroll
        for (int e = 0; e < 4; ++e) t[bb * 4 + e] = tr_step<ROR4>(zz[bb][e], zz[bb][e + 4], lane & 4);
#pragma unroll
      for (int bb = 0; bb < 2; ++bb)
#pragma unroll
        for (int e = 0; e < 2; ++e) u[bb * 2 + e] = tr_step<ROR8>(t[bb * 4 + e], t[bb * 4 + e + 2], lane & 8);
      v2[0] = swap16_add(u[0], u[1]); v2[1] = swap16_add(u[2], u[3]);
      const float z = swap32_add(v2[0], v2[1]);
      const int col = 8 * (lane & 3) + ((lane >> 2) & 1) * 4 + ((lane >> 3) & 1) * 2 + ((lane >> 4) & 1);
      L.z[w][(lane >> 5) * 32 + col] = z;
    }
    WG_BARRIER();
    u32 gp[2][2];
    {
      float z = 0.f;
#pragma unroll
      for (int i = 0; i < 8; ++i) z += L.z[i][lane];
      z = rbf(rbf(z) + bf_lo(b1raw));
      const float uu = 0.7978845608028654f * (z + 0.044715f * z * z * z);
      const float th = 1.f - 2.f / (1.f + ex2(2.f * LOG2E * uu));
      const float g = 0.5f * z * (1.f + th);
      const float gn = dpp<XOR1>(g);
      if ((lane & 1) == 0) L.g[w][lane >> 1] = pack_bf16(g, gn);
      LDS_SYNC();
#pragma unroll
      for (int bb = 0; bb < 2; ++bb) { gp[bb][0] = L.g[w][bb * 16 + r8]; gp[bb][1] = L.g[w][bb * 16 + 8 + r8]; }
    }
    {
      float y[2][8];
#pragma unroll
      for (int bb = 0; bb < 2; ++bb)
#pragma unroll
        for (int e = 0; e < 8; ++e) y[bb][e] = dot2(P2[1][e], gp[bb][1], dot2(P2[0][e], gp[bb][0], 0.f));
      float t[8], u[4], v2[2];
#pragma unroll
      for (int bb = 0; bb < 2; ++bb)
#pragma unroll
        for (int e = 0; e < 4; ++e) t[bb * 4 + e] = tr_step<ROR8>(y[bb][e], y[bb][e + 4], lane & 8);
#pragma unroll
      for (int bb = 0; bb < 2; ++bb)
#pragma unroll
        for (int e = 0; e < 2; ++e) u[bb * 2 + e] = swap16_add(t[bb * 4 + e], t[bb * 4 + e + 2]);
      v2[0] = swap32_add(u[0], u[2]); v2[1] = swap32_add(u[1], u[3]);
      const u32 unit = w * 32 + c8 * 4 + ((lane >> 3) & 1) * 2 + ((lane >> 4) & 1);
      st_unit(rws, WS_F + ((fid * 2 + (lane >> 5)) * 256 + unit) * 16, asu(v2[0]), asu(v2[1]), tag);
    }
    STAMP(8);
  }
}

__global__ void tiny(unsigned char* ws) { *reinterpret_cast<unsigned long long*>(ws + WS_TS) = __builtin_amdgcn_s_memrealtime(); }
extern "C" void kernel_launch(void* const* d_in, const int* in_sizes, int n_in, void* d_out, int out_size, void* d_ws,
                              size_t ws_size, hipStream_t stream) {
  tiny<<<1, 64, 0, stream>>>((unsigned char*)d_ws);
  fused_block<<<256, 512, 0, stream>>>((const u16*)d_in[0], (const u16*)d_in[1], (const u16*)d_in[2], (const u16*)d_in[3],
                                       (const u16*)d_in[4], (const u16*)d_in[6], (const u16*)d_in[7], (const u16*)d_in[8],
                                       (const u16*)d_in[9], (u16*)d_out, (unsigned char*)d_ws);
}
```

```cpp
#include <hip/hip_runtime.h>
#include <stdint.h>

typedef unsigned short u16;
typedef unsigned int u32;
typedef u32 u32x4 __attribute__((ext_vector_type(4)));
typedef u32 u32x2 __attribute__((ext_vector_type(2)));
typedef float f32x4 __attribute__((ext_vector_type(4)));
typedef float f32x2 __attribute__((ext_vector_type(2)));
typedef __bf16 bf16x2 __attribute__((ext_vector_type(2)));
typedef __bf16 bf16x8 __attribute__((ext_vector_type(8)));
typedef __amdgpu_buffer_rsrc_t rsrc_t;

constexpr int WA = 6;
constexpr int WB = 3;
constexpr int WC = 2;
constexpr int LEAD = 40;
constexpr int SA = 60;
constexpr int SBT = 286;
constexpr int TB = 90;
constexpr int QPOS = 26;
constexpr int QT = 680;
constexpr int MLT = 60;
constexpr int KB0 = 2;
constexpr int FSLEEP = 24, PSLEEP = 1;

constexpr int NB = 2, DM = 512, NH = 32, SK = 4096, DH = 128, FF = 1376;
constexpr int NFW = 43;
constexpr int AUX_NT = 2, AUX_SC1 = 16;
constexpr u32 OOB = 0x7ffffff0u;
constexpr u32 WS_E = 0;
constexpr u32 WS_T = 1024;
constexpr u32 WS_Q = 4096;
constexpr u32 WS_ML = WS_Q + 32 * 2 * 4 * 64 * 16;
constexpr u32 WS_H = WS_ML + 256 * 128;
constexpr u32 WS_BY = WS_H + 128 * 128;
constexpr u32 WS_Y = WS_BY + 256 * 128;
constexpr u32 WS_F = WS_Y + 2 * 128 * 256 * 16;
constexpr u32 WS_END = WS_F + NFW * 512 * 16;
constexpr u32 SPIN_CAP = 1u << 22;

#define SB() __builtin_amdgcn_sched_barrier(0)
#define WAITVM_N(n) asm volatile("s_waitcnt vmcnt(%0)" ::"n"(n) : "memory")
#define LDS_SYNC() do { asm volatile("s_waitcnt lgkmcnt(0)" ::: "memory"); } while (0)
#define WG_BARRIER() do { asm volatile("s_waitcnt lgkmcnt(0)\n\ts_barrier" ::: "memory"); } while (0)

__device__ __forceinline__ float asf(u32 v) { return __builtin_bit_cast(float, v); }
__device__ __forceinline__ u32 asu(float v) { return __builtin_bit_cast(u32, v); }
__device__ __forceinline__ float bf_lo(u32 v) { return asf(v << 16); }
__device__ __forceinline__ float bf_hi(u32 v) { return asf(v & 0xffff0000u); }
__device__ __forceinline__ u32 pack_bf16(float lo, float hi) {
  return __builtin_bit_cast(u32, __builtin_convertvector(f32x2{lo, hi}, bf16x2));
}
__device__ __forceinline__ float rbf(float x) { return asf(pack_bf16(0.f, x)); }
__device__ __forceinline__ float dot2(u32 a, u32 b, float c) {
  return __builtin_amdgcn_fdot2_f32_bf16(__builtin_bit_cast(bf16x2, a), __builtin_bit_cast(bf16x2, b), c, false);
}
__device__ __forceinline__ f32x4 mfma16(u32x4 a, u32x4 b, f32x4 c) {
  return __builtin_amdgcn_mfma_f32_16x16x32_bf16(__builtin_bit_cast(bf16x8, a), __builtin_bit_cast(bf16x8, b), c, 0, 0, 0);
}
template <int CTRL> __device__ __forceinline__ float dpp(float x) {
  return asf((u32)__builtin_amdgcn_mov_dpp((int)asu(x), CTRL, 0xf, 0xf, true));
}
template <int CTRL> __device__ __forceinline__ u32 dppu(u32 x) { return (u32)__builtin_amdgcn_mov_dpp((int)x, CTRL, 0xf, 0xf, true); }
constexpr int XOR1 = 0xB1, XOR2 = 0x4E, HMIR = 0x141, MIR = 0x140, ROR4 = 0x124, ROR8 = 0x128;
__device__ __forceinline__ float swap16_add(float a, float b) {
  auto r = __builtin_amdgcn_permlane16_swap(asu(a), asu(b), false, false);
  return asf(r[0]) + asf(r[1]);
}
__device__ __forceinline__ float swap32_add(float a, float b) {
  auto r = __builtin_amdgcn_permlane32_swap(asu(a), asu(b), false, false);
  return asf(r[0]) + asf(r[1]);
}
__device__ __forceinline__ float swap16_max(float a) {
  auto r = __builtin_amdgcn_permlane16_swap(asu(a), asu(a), false, false);
  return fmaxf(asf(r[0]), asf(r[1]));
}
__device__ __forceinline__ float swap32_max(float a) {
  auto r = __builtin_amdgcn_permlane32_swap(asu(a), asu(a), false, false);
  return fmaxf(asf(r[0]), asf(r[1]));
}
__device__ __forceinline__ float row_sum(float x) {
  x += dpp<XOR1>(x); x += dpp<XOR2>(x); x += dpp<HMIR>(x); x += dpp<MIR>(x);
  return x;
}
__device__ __forceinline__ float wave_sum(float x) {
  x = row_sum(x);
  x = swap16_add(x, x); x = swap32_add(x, x);
  return x;
}
__device__ __forceinline__ float wave_max(float x) {
  x = fmaxf(x, dpp<XOR1>(x)); x = fmaxf(x, dpp<XOR2>(x)); x = fmaxf(x, dpp<HMIR>(x)); x = fmaxf(x, dpp<MIR>(x));
  x = swap16_max(x); x = swap32_max(x);
  return x;
}
template <int CTRL> __device__ __forceinline__ float tr_step(float a, float b, bool sel) {
  const float keep = sel ? b : a, give = sel ? a : b;
  return keep + dpp<CTRL>(give);
}
__device__ __forceinline__ rsrc_t mk_rsrc(const void* p, u32 bytes) {
  return __builtin_amdgcn_make_buffer_rsrc(const_cast<void*>(p), (short)0, (int)bytes, 0x00020000);
}
template <int AUX> __device__ __forceinline__ u32x4 ld128(rsrc_t r, u32 voff, u32 soff) {
  return __builtin_amdgcn_raw_buffer_load_b128(r, (int)voff, (int)soff, AUX);
}
template <int AUX> __device__ __forceinline__ u32 ld32(rsrc_t r, u32 voff, u32 soff) {
  return __builtin_amdgcn_raw_buffer_load_b32(r, (int)voff, (int)soff, AUX);
}
__device__ __forceinline__ void st_unit(rsrc_t r, u32 off, u32 a, u32 b, u32 tag) {
  __builtin_amdgcn_raw_buffer_store_b128(u32x4{a, tag, b, tag}, r, (int)off, 0, AUX_SC1);
}
__device__ __forceinline__ u32 unit_bad(u32x4 v, u32 tag) { return (v[1] ^ tag) | (v[3] ^ tag); }
__device__ __forceinline__ float ex2(float x) { return __builtin_amdgcn_exp2f(x); }
__device__ __forceinline__ float rcp(float x) { return __builtin_amdgcn_rcpf(x); }
__device__ __forceinline__ unsigned long long now() { return __builtin_amdgcn_s_memrealtime(); }
constexpr float LOG2E = 1.4426950408889634f;

__device__ __forceinline__ float ln_rden(float m1, float m2) {
  return rcp(rbf(__builtin_amdgcn_sqrtf(rbf(rbf(fmaxf(fmaf(-m1, m1, m2), 0.f)) + 1e-5f))));
}
__device__ __forceinline__ void ln_stats2(const float (&v)[2][8], int lane, float (&mb)[2], float (&rd)[2]) {
  float c[2], s[2], q[2];
#pragma unroll
  for (int bb = 0; bb < 2; ++bb) {
    c[bb] = asf(__builtin_amdgcn_readfirstlane(asu(v[bb][0])));
    s[bb] = 0.f; q[bb] = 0.f;
#pragma unroll
    for (int i = 0; i < 8; ++i) { const float d = v[bb][i] - c[bb]; s[bb] += d; q[bb] = fmaf(d, d, q[bb]); }
  }
  const float ss = swap32_add(s[0], s[1]), qq = swap32_add(q[0], q[1]);
  const float t = row_sum(swap16_add(ss, qq));
  auto r = __builtin_amdgcn_permlane16_swap(asu(t), asu(t), false, false);
  const float m1 = asf(r[0]) * (1.f / DM), m2 = asf(r[1]) * (1.f / DM);
  const float mbv = rbf((lane < 32 ? c[0] : c[1]) + m1);
  const float rdv = ln_rden(m1, m2);
  auto a = __builtin_amdgcn_permlane32_swap(asu(mbv), asu(mbv), false, false);
  auto b = __builtin_amdgcn_permlane32_swap(asu(rdv), asu(rdv), false, false);
  mb[0] = asf(a[0]); mb[1] = asf(a[1]); rd[0] = asf(b[0]); rd[1] = asf(b[1]);
}
__device__ __forceinline__ float sum4(f32x4 v) { const f32x2 t = f32x2{v[0], v[1]} + f32x2{v[2], v[3]}; return t[0] + t[1]; }
__device__ __forceinline__ float ln_pre(float x, float mb, float rd) { return rbf(x - mb) * rd; }

struct Lds {
  u32x4 w1f[64][37];
  float z[3][64][4];
  u32 ha[4][3][64];
  u32 g[4][32];
  u32 oa[8][68];
  float p[8][4][32];
  float red[96];
  u32 xn[2][256];
  u16 xr[2][512];
  u32 bias[64];
  u32 q[64];
  float ml[8][2];
  float scale[12];
  u32 flag[16];
};

typedef __attribute__((address_space(3))) unsigned char* lds_p;
#define LDS_AT(T, p, o) (*(__attribute__((address_space(3))) T*)((p) + (o)))

template <int I> struct IC { static constexpr int v = I; };
template <int LO, int HI, class F> __device__ __forceinline__ void static_for(F&& f) {
  if constexpr (LO < HI) { f(IC<LO>{}); static_for<LO + 1, HI>(f); }
}
__device__ __forceinline__ void perm_pairs(u32x4 a, u32x4 b, u32 (&P)[8]) {
#pragma unroll
  for (int e = 0; e < 4; ++e) {
    P[2 * e] = __builtin_amdgcn_perm(b[e], a[e], 0x05040100u);
    P[2 * e + 1] = __builtin_amdgcn_perm(b[e], a[e], 0x07060302u);
  }
}
template <int SLEEP> __device__ __forceinline__ u32x4 poll_unit(rsrc_t r, u32 off, u32 tag) {
  u32x4 v;
  u32 spins = SLEEP > 8 ? SPIN_CAP >> 3 : SPIN_CAP;
  for (;;) {
    asm volatile("" : "+s"(spins)::"memory");
    v = ld128<AUX_SC1>(r, off, 0);
    if (__builtin_amdgcn_ballot_w64(unit_bad(v, tag) != 0u) == 0ull || --spins == 0u) break;
    if (SLEEP) __builtin_amdgcn_s_sleep(SLEEP);
  }
  return v;
}
__device__ __forceinline__ u32 poll_granule(rsrc_t r, u32 off, u32 tag) {
  u32x2 v;
  u32 spins = SPIN_CAP >> 1;
  for (;;) {
    v = u32x2{0u, 0u};
    asm volatile("buffer_atomic_or_x2 %0, %1, %2, 0 offen sc0 sc1\n\ts_waitcnt vmcnt(0)" : "+v"(v) : "v"(off), "s"(r) : "memory");
    if (__builtin_amdgcn_ballot_w64(v[1] != tag) == 0ull || --spins == 0u) break;
  }
  return v[0];
}
__device__ __forceinline__ void lds_wait(const u32* p) {
  typedef const volatile __attribute__((address_space(3))) u32* lptr;
  const lptr lp = (lptr)p;
  u32 spins = 0;
  while (!__all(*lp != 0u) && ++spins < (SPIN_CAP << 2)) __builtin_amdgcn_s_sleep(2);
  asm volatile("" ::: "memory");
}

constexpr int NSEQ = 80;
constexpr int seq_type(int t) { return t < 32 ? 0 : (t < 48 ? 1 : ((t & 1) ? 1 : 2)); }
constexpr int seq_idx(int t) { return t < 32 ? t : (t < 48 ? t - 32 : ((t & 1) ? 16 + (t - 48) / 2 : (t - 48) / 2)); }
constexpr int pace_n(int t, int W) {
  int n = 0;
  for (int u = t - 1; u >= 0; --u) { if (seq_type(u) != 2 && ++n == W) return t - 1 - u; }
  return -1;
}
constexpr int sched(int t) {
  int n = 0;
  for (int u = 0; u < t; ++u) n += seq_type(u) == 2 ? 22 : 100;
  return SA + n * SBT / 1000;
}
constexpr int T0 = QPOS + WA;
constexpr int cons_end(int t) {
  if (t < T0 + 9) return 32;
  const int lim = t >= NSEQ ? NSEQ : t - pace_n(t, WA);
  const int cu = 32 + 2 * (t - (T0 + 9) + 1);
  return cu < lim ? cu : lim;
}

__global__ void __launch_bounds__(576, 3)
fused_block(const u16* __restrict__ x, const u16* __restrict__ kc, const u16* __restrict__ vc, const u16* __restrict__ wq,
            const u16* __restrict__ wo, const u16* __restrict__ w1, const u16* __restrict__ b1, const u16* __restrict__ w2,
            const u16* __restrict__ b2, u16* __restrict__ out, unsigned char* __restrict__ ws) {
  __shared__ __attribute__((aligned(16))) Lds L;
  const int tid = threadIdx.x, lane = tid & 63, wv = __builtin_amdgcn_readfirstlane(tid >> 6), w = wv - 1;
  const int r = lane >> 4, c = lane & 15, r8 = lane >> 3, c8 = lane & 7;
  const int bid = blockIdx.x, xcd = bid & 7, jj = bid >> 3;
  const int h = xcd * 4 + (jj >> 3), mem = jj & 7, b = mem >> 2, s = mem & 3;
  const int fid = xcd * 6 + jj, rid = xcd * 16 + (jj & 15);
  const bool is_red = jj >= 16, is_ffn = jj < 6 && fid < NFW;
  asm volatile("" : "+s"(x), "+s"(kc), "+s"(vc), "+s"(wq), "+s"(wo), "+s"(w1), "+s"(b1), "+s"(w2), "+s"(b2), "+s"(out), "+s"(ws));
  const unsigned long long ts0 = now();
  const rsrc_t rws = mk_rsrc(ws, WS_END);
  const rsrc_t rout = mk_rsrc(out, NB * DM * 2);

  if (wv == 0) {
    __builtin_amdgcn_s_setprio(3);
    if (lane < 16) L.flag[lane] = 0u;
    WG_BARRIER();
    const rsrc_t rx = mk_rsrc(x, NB * DM * 2);
    const rsrc_t rq = mk_rsrc(wq, DM * NH * DH * 2);
    u32x4 xv[2], wqv[16];
    xv[0] = ld128<0>(rx, lane * 16, 0);
    xv[1] = ld128<0>(rx, lane * 16, 1024);
    const u32 epoch = ld32<AUX_SC1>(rws, 0, WS_E);
    const int kq = mem >> 1, hf = mem & 1;
    const u32 qvoff = (128 * kq + 2 * r8) * 8192 + h * 256 + hf * 128 + c8 * 16;
#pragma unroll
    for (int i = 0; i < 16; ++i) wqv[i] = ld128<AUX_NT>(rq, qvoff, ((i >> 1) * 16 + (i & 1)) * 8192);
    const u32 bld = __builtin_amdgcn_raw_buffer_load_b16(is_red ? mk_rsrc(b2, DM * 2) : mk_rsrc(b1, FF * 2),
                                                         is_red ? (rid * 4 + (lane & 3)) * 2 : (is_ffn ? (fid * 32 + (lane & 31)) * 2 : OOB), 0, 0);
    const u32 otouch = ld32<0>(rout, is_red ? rid * 8 : OOB, 0);
    SB();
    {
      float v[2][8], mb[2], rd[2];
#pragma unroll
      for (int bb = 0; bb < 2; ++bb)
#pragma unroll
        for (int i = 0; i < 4; ++i) { v[bb][2 * i] = bf_lo(xv[bb][i]); v[bb][2 * i + 1] = bf_hi(xv[bb][i]); }
      ln_stats2(v, lane, mb, rd);
#pragma unroll
      for (int bb = 0; bb < 2; ++bb) {
        u32x4 n;
#pragma unroll
        for (int i = 0; i < 4; ++i) n[i] = pack_bf16(ln_pre(v[bb][2 * i], mb[bb], rd[bb]), ln_pre(v[bb][2 * i + 1], mb[bb], rd[bb]));
        *reinterpret_cast<u32x4*>(&L.xn[bb][4 * lane]) = n;
        *reinterpret_cast<u32x4*>(&L.xr[bb][8 * lane]) = xv[bb];
      }
    }
    const u32 tag = __builtin_amdgcn_readfirstlane(epoch) + 1u;
    LDS_SYNC();
    {
      float z0[8], z1[8];
#pragma unroll
      for (int e = 0; e < 8; ++e) { z0[e] = 0.f; z1[e] = 0.f; }
#pragma unroll
      for (int j = 0; j < 8; ++j) {
        const u32 n0 = L.xn[0][64 * kq + 8 * j + r8], n1 = L.xn[1][64 * kq + 8 * j + r8];
        u32 P[8];
        perm_pairs(wqv[2 * j], wqv[2 * j + 1], P);
#pragma unroll
        for (int e = 0; e < 8; ++e) { z0[e] = dot2(P[e], n0, z0[e]); z1[e] = dot2(P[e], n1, z1[e]); }
      }
      float t[8], u[4];
#pragma unroll
      for (int e = 0; e < 8; ++e) t[e] = swap32_add(z0[e], z1[e]);
#pragma unroll
      for (int e = 0; e < 4; ++e) u[e] = swap16_add(t[e], t[e + 4]);
      const float f0 = tr_step<ROR8>(u[0], u[2], lane & 8), f1 = tr_step<ROR8>(u[1], u[3], lane & 8);
      const u32 unit = hf * 32 + c8 * 4 + ((lane >> 4) & 1) * 2 + ((lane >> 3) & 1);
      st_unit(rws, WS_Q + (((h * 2 + (lane >> 5)) * 4 + kq) * 64 + unit) * 16, asu(f0), asu(f1), tag);
    }
    st_unit(rws, (jj == 0 && lane == 0) ? WS_T + xcd * 16 : OOB, (u32)ts0, 0u, tag);
    L.bias[lane] = bld << 16;
    asm volatile("" ::"v"(otouch));
    {
      const u32 qoff = WS_Q + ((h * 2 + b) * 4 * 64 + lane) * 16;
      u32x4 qg[4], tg;
      u32 spins = 0;
      while (now() - ts0 < QT) __builtin_amdgcn_s_sleep(2);
      for (;;) {
        asm volatile("" ::: "memory");
#pragma unroll
        for (int n = 0; n < 4; ++n) qg[n] = ld128<AUX_SC1>(rws, qoff, n * 1024);
        tg = ld128<AUX_SC1>(rws, WS_T + c8 * 16, 0);
        asm volatile("" : "+v"(tg));
        if (__all((unit_bad(qg[0], tag) | unit_bad(qg[1], tag) | unit_bad(qg[2], tag) | unit_bad(qg[3], tag)) == 0u) || ++spins >= (SPIN_CAP >> 3)) break;
        __builtin_amdgcn_s_sleep(32);
      }
      L.q[lane] = pack_bf16((asf(qg[0][0]) + asf(qg[1][0])) + (asf(qg[2][0]) + asf(qg[3][0])), (asf(qg[0][2]) + asf(qg[1][2])) + (asf(qg[2][2]) + asf(qg[3][2])));
      int lag = unit_bad(tg, tag) == 0u ? (int)((u32)ts0 - tg[0]) : 0;
      lag = max(lag, (int)dppu<XOR1>((u32)lag)); lag = max(lag, (int)dppu<XOR2>((u32)lag)); lag = max(lag, (int)dppu<HMIR>((u32)lag));
      lag = min(max(lag, 0), 200);
      if (lane == 0) { L.flag[9] = tag; L.flag[12] = (u32)ts0 - (u32)lag; }
      LDS_SYNC();
      if (lane == 0) L.flag[8] = 1u;
    }
    lds_wait(&L.flag[lane & 7]);
    {
      const f32x2 mlw = *reinterpret_cast<const f32x2*>(&L.ml[lane & 7][0]);
      float M = fmaxf(mlw[0], dpp<XOR1>(mlw[0])); M = fmaxf(M, dpp<XOR2>(M)); M = fmaxf(M, dpp<HMIR>(M));
      float Lw = mlw[1] * ex2((mlw[0] - M) * LOG2E);
      Lw += dpp<XOR1>(Lw); Lw += dpp<XOR2>(Lw); Lw += dpp<HMIR>(Lw);
      st_unit(rws, lane < 8 ? WS_ML + ((b * 32 + h) * 4 + s) * 128 + lane * 16 : OOB, asu(M), asu(Lw), tag);
      lds_wait(&L.flag[10]);
      const u32x4 mlv = poll_unit<32>(rws, WS_ML + ((b * 32 + h) * 4 + (lane & 3)) * 128, tag);
      const float ms = asf(mlv[0]), ls = asf(mlv[2]);
      float G = fmaxf(ms, dpp<XOR1>(ms)); G = fmaxf(G, dpp<XOR2>(G));
      float Ls = ls * ex2((ms - G) * LOG2E);
      Ls += dpp<XOR1>(Ls); Ls += dpp<XOR2>(Ls);
      const float scl = ex2((mlw[0] - G) * LOG2E) * rcp(Ls);
      if (lane < 12) L.scale[lane] = lane < 8 ? scl : 0.f;
    }
    WG_BARRIER();
    st_unit(rws, lane < 8 ? WS_BY + (b * 128 + h * 4 + s) * 128 + lane * 16 : OOB, 0u, 0u, tag);
    return;
  }

  const rsrc_t rk = mk_rsrc(kc + (size_t)(b * NH + h) * SK * DH, SK * DH * 2);
  const rsrc_t rv = mk_rsrc(vc + (size_t)(b * NH + h) * SK * DH, SK * DH * 2);
  const rsrc_t ro = mk_rsrc(wo + (size_t)h * DH * DM, DH * DM * 2);
  const u32 kvoff = lane * 16;
  const u32 kvbase = (s * 8 + w) * 2048 + (h & 3) * 262144;
  const u32 ovoff = r8 * 2048 + c8 * 16 + w * 128;
#define KVOFF(i) (kvbase ^ (((i) >> 1) * 65536 + ((i) & 1) * 1024))
  u32x4 K[32], V[32], WO[16];
  u32x4 OB[8][2];
  u32 tref = (u32)ts0;
  int late = -1;
  auto issue = [&](auto tc) {
    constexpr int t = decltype(tc)::v, i = seq_idx(t);
    if constexpr (seq_type(t) != 2) {
      if constexpr ((i & 3) == 0 && t >= 8) late = (int)((u32)now() - tref - (u32)sched(t));
      constexpr int na = pace_n(t, WA), nb = pace_n(t, WB), nc = pace_n(t, WC);
      if constexpr (na >= 0) WAITVM_N(na);
      if constexpr (WC < WB && t >= 8)
        asm volatile("s_cmp_ge_i32 %0, 0\n\ts_cbranch_scc1 1f\n\ts_waitcnt vmcnt(%1)\n\ts_cmp_ge_i32 %0, %3\n\ts_cbranch_scc1 1f\n\ts_waitcnt vmcnt(%2)\n1:" ::"s"(late), "n"(nb), "n"(nc), "n"(-LEAD) : "scc", "memory");
      else if constexpr (WB < WA && nb >= 0)
        asm volatile("s_cmp_ge_i32 %0, 0\n\ts_cbranch_scc1 1f\n\ts_waitcnt vmcnt(%1)\n1:" ::"s"(late), "n"(nb) : "scc", "memory");
    }
    if constexpr (seq_type(t) == 0) K[i] = ld128<AUX_NT>(rk, kvoff, KVOFF(i));
    else if constexpr (seq_type(t) == 1) V[i] = ld128<AUX_NT>(rv, kvoff, KVOFF(i));
    else WO[i] = ld128<AUX_NT>(ro, ovoff, ((i >> 1) * 16 + (i & 1)) * 1024);
    SB();
  };
  static_for<0, KB0>(issue);
  WG_BARRIER();
  static_for<KB0, T0>(issue);
  lds_wait(&L.flag[8]);
  const u32x4 q = *reinterpret_cast<const u32x4*>(&L.q[4 * c]);
  const u32 tag = __builtin_amdgcn_readfirstlane(L.flag[9]);
  tref = __builtin_amdgcn_readfirstlane(L.flag[12]);
  float sc[2], t3[2], t4[2];
  static_for<0, 8>([&](auto kc_) {
    constexpr int k = decltype(kc_)::v;
    issue(IC<T0 + k>{});
    float d[4];
#pragma unroll
    for (int n = 0; n < 4; ++n) {
      d[n] = dot2(K[4 * k + n][0], q[0], 0.f);
      d[n] = dot2(K[4 * k + n][1], q[1], d[n]); d[n] = dot2(K[4 * k + n][2], q[2], d[n]); d[n] = dot2(K[4 * k + n][3], q[3], d[n]);
    }
    t3[k & 1] = tr_step<XOR2>(tr_step<XOR1>(d[0], d[1], c & 1), tr_step<XOR1>(d[2], d[3], c & 1), c & 2);
    if constexpr ((k & 1) == 1) {
      t4[(k >> 1) & 1] = tr_step<ROR4>(t3[0], t3[1], c & 4);
      if constexpr ((k & 3) == 3) sc[k >> 2] = tr_step<ROR8>(t4[0], t4[1], c & 8);
    }
    asm volatile("" : "+v"(t3[k & 1]));
    SB();
  });
  issue(IC<T0 + 8>{});
  {
    float s0 = rbf(rbf(sc[0]) * 0.08838834764831845f), s1 = rbf(rbf(sc[1]) * 0.08838834764831845f);
    const float mw = wave_max(fmaxf(s0, s1));
    float p0 = ex2((s0 - mw) * LOG2E), p1 = ex2((s1 - mw) * LOG2E);
    L.p[w][r][c] = p0; L.p[w][r][16 + c] = p1;
    const float lw = wave_sum(p0 + p1);
    *reinterpret_cast<f32x2*>(&L.ml[w][0]) = f32x2{mw, lw};
    LDS_SYNC();
    L.flag[w] = 1u;
  }
  SB();
  u32 xcol, bcol, yoff, boff, goff0, goff1, hoff, ooff, eoff, poff;
  lds_p a_ow, a_or, a_sc, rs0, rs1, rrd;
  const int pit = (7 - w) * 10 + jj - 6;
  const rsrc_t rp = pit < 32 ? mk_rsrc(w1, DM * FF * 2) : mk_rsrc(w2, FF * DM * 2);
  float acc[8];
#pragma unroll
  for (int k = 0; k < 8; ++k) acc[k] = 0.f;
  f32x4 pv;
  auto consume = [&](auto tc) {
    constexpr int t = decltype(tc)::v, i = seq_idx(t);
    if constexpr (seq_type(t) == 1) {
      if constexpr ((i & 3) == 0) pv = *reinterpret_cast<const f32x4*>(&L.p[w][r][i]);
      const float p = pv[i & 3];
#pragma unroll
      for (int e = 0; e < 4; ++e) { acc[2 * e] = fmaf(p, bf_lo(V[i][e]), acc[2 * e]); acc[2 * e + 1] = fmaf(p, bf_hi(V[i][e]), acc[2 * e + 1]); }
      asm volatile("" : "+v"(acc[0]), "+v"(acc[1]), "+v"(acc[2]), "+v"(acc[3]), "+v"(acc[4]), "+v"(acc[5]), "+v"(acc[6]), "+v"(acc[7]));
    } else if constexpr ((i & 1) == 1) {
#pragma unroll
      for (int e = 0; e < 4; ++e) {
        u32 lo = __builtin_amdgcn_perm(WO[i][e], WO[i - 1][e], 0x05040100u), hi = __builtin_amdgcn_perm(WO[i][e], WO[i - 1][e], 0x07060302u);
        asm volatile("" : "+v"(lo), "+v"(hi));
        OB[2 * e][i >> 3][(i >> 1) & 3] = lo; OB[2 * e + 1][i >> 3][(i >> 1) & 3] = hi;
      }
    }
    SB();
  };
  static_for<T0 + 9, NSEQ>([&](auto tc) {
    constexpr int t = decltype(tc)::v;
    issue(tc);
    if constexpr (t == MLT) { L.flag[w == 7 ? 10 : 11] = 1u; }
    if constexpr (t == T0 + 20) {
      yoff = (lane & 8) ? OOB : WS_Y + ((b * 128 + h * 4 + s) * 256 + w * 32 + c8 * 4 + (lane >> 5) * 2 + ((lane >> 4) & 1)) * 16;
      xcol = (u32)L.xr[(lane >> 2) & 1][rid * 4 + (lane & 3)] << 16; bcol = L.bias[lane];
      boff = WS_BY + (32 * w + (lane >> 1)) * 128;
      goff0 = WS_Y + ((32 * w + (lane >> 1)) * 256 + rid * 2 + (lane & 1)) * 16;
      goff1 = 8 * w + r8 >= NFW ? OOB : WS_F + ((8 * w + r8) * 512 + ((rid >> 5) * 2 + (rid & 1)) * 64 + ((rid & 31) >> 1) * 4) * 16 + c8 * 8;
      hoff = (lane & 35) == 0 ? WS_H + rid * 128 + (lane >> 2) * 16 : OOB;
      ooff = (lane & 57) == 0 ? ((lane >> 2) * DM + rid * 4 + (lane & 2)) * 2 : OOB;
      eoff = (rid == 0 && lane == 0) ? WS_E : OOB;
      rs0 = (lds_p)&L.red[(w & 3) * 8 + (w >> 2) * 4 + (lane & 1) * 2 + (lane >> 5)];
      rs1 = (lds_p)&L.red[32 + w * 8 + (lane & 1) * 4 + (c8 >> 1)];
      rrd = (lds_p)&L.red[lane & 31];
      const int pidx = 4 * c + 2 * (r & 1) + (r >> 1);
      a_ow = (lds_p)&L.oa[w][(pidx & 7) * 8 + (pidx >> 3)];
      a_or = (lds_p)&L.oa[c8][r8 * 8];
      a_sc = (lds_p)&L.scale[(r >> 1) == ((lane >> 3) & 1) ? 4 * (r & 1) : 8];
      asm volatile("" : "+v"(a_ow), "+v"(a_or), "+v"(a_sc));
      asm volatile("" : "+v"(xcol), "+v"(bcol), "+v"(yoff), "+v"(boff), "+v"(goff0), "+v"(goff1));
      poff = (jj < 6 || is_red || pit >= 56) ? OOB
           : pit < 32 ? (16 * pit + (lane >> 2)) * (FF * 2) + 384 * xcd + ((lane & 3) == 3 ? 380 : (lane & 3) * 128)
                      : (192 * xcd + 8 * (pit - 32) + (lane >> 3)) * 1024 + (lane & 7) * 128;
      asm volatile("" : "+v"(hoff), "+v"(ooff), "+v"(eoff), "+v"(rs0), "+v"(rs1), "+v"(rrd), "+v"(poff));
    }
    static_for<cons_end(t - 1), cons_end(t)>(consume);
  });
  static_for<cons_end(NSEQ - 1), NSEQ>(consume);
  {
    float t[4], u[2];
#pragma unroll
    for (int e = 0; e < 4; ++e) t[e] = swap16_add(acc[e], acc[e + 4]);
#pragma unroll
    for (int e = 0; e < 2; ++e) u[e] = swap32_add(t[e], t[e + 2]);
    LDS_AT(u32, a_ow, 0) = pack_bf16(u[0], u[1]);
  }
  WG_BARRIER();
  {
    const u32x4 a0 = LDS_AT(u32x4, a_or, 0), a1 = LDS_AT(u32x4, a_or, 16);
    const f32x4 sc4 = LDS_AT(f32x4, a_sc, 0);
    SB();
    f32x4 d[8];
#pragma unroll
    for (int n = 0; n < 8; ++n) d[n] = mfma16(a0, OB[n][0], f32x4{0.f, 0.f, 0.f, 0.f});
    SB();
#pragma unroll
    for (int n = 0; n < 8; ++n) d[n] = mfma16(a1, OB[n][1], d[n]);
    SB();
    float y[8];
#pragma unroll
    for (int n = 0; n < 8; ++n) {
      const f32x2 p = f32x2{d[n][0], d[n][1]} * f32x2{sc4[0], sc4[1]} + f32x2{d[n][2], d[n][3]} * f32x2{sc4[2], sc4[3]};
      y[n] = p[0] + p[1];
    }
    float t[4], u[2];
#pragma unroll
    for (int e = 0; e < 4; ++e) t[e] = swap32_add(y[e], y[e + 4]);
#pragma unroll
    for (int e = 0; e < 2; ++e) u[e] = swap16_add(t[e], t[e + 2]);
    u[0] += dpp<ROR8>(u[0]); u[1] += dpp<ROR8>(u[1]);
    st_unit(rws, yoff, asu(u[0]), asu(u[1]), tag);
  }

  if (__builtin_expect(is_red, 1)) {
    while ((u32)now() - tref < (u32)(sched(NSEQ) + TB)) { }
    poll_unit<PSLEEP>(rws, boff, tag);
    {
      const u32x4 v = poll_unit<PSLEEP>(rws, goff0, tag);
      float a = swap32_add(asf(v[0]), asf(v[2]));
      a = swap16_add(a, a);
      a += dpp<ROR8>(a); a += dpp<ROR4>(a); a += dpp<XOR2>(a);
      LDS_AT(float, rs0, 0) = a;
    }
    WG_BARRIER();
    float hcol;
    if (w == 0) {
      float sum = LDS_AT(float, rrd, 0);
      sum += dpp<ROR8>(sum);
      sum = swap16_add(sum, sum);
      hcol = rbf(asf(xcol) + rbf(sum));
      const u32 hp = pack_bf16(hcol, dpp<XOR1>(hcol));
      st_unit(rws, hoff, hp, dppu<XOR2>(hp), tag);
      __builtin_amdgcn_raw_buffer_store_b32(tag, rws, (int)eoff, 0, AUX_SC1);
    }
    __builtin_amdgcn_s_sleep(FSLEEP);
    {
      float a = 0.f;
      if (w < 6) a = asf(poll_granule(rws, goff1, goff1 == OOB ? 0u : tag));
      a = swap32_add(a, a); a = swap16_add(a, a);
      a += dpp<ROR8>(a);
      LDS_AT(float, rs1, 0) = a;
    }
    WG_BARRIER();
    if (w == 0) {
      float sum = L.red[32 + lane];
      sum += dpp<ROR8>(sum);
      sum = swap16_add(sum, sum); sum = swap32_add(sum, sum);
      const float o = hcol + rbf(rbf(sum) + asf(bcol));
      __builtin_amdgcn_raw_buffer_store_b32(pack_bf16(o, dpp<XOR1>(o)), rout, (int)ooff, 0, AUX_NT);
    }
  } else if (is_ffn) {
    const bool stage = w < 4;
    const int w4 = w & 3;
    u32x4 W[8], B2[8];
    asm volatile("s_barrier" ::: "memory");
    {
      const rsrc_t rw = stage ? mk_rsrc(w1, DM * FF * 2) : mk_rsrc(w2, FF * DM * 2);
      const u32 voff = stage ? (128 * w4 + 8 * (lane >> 2)) * (FF * 2) + fid * 64 + (lane & 3) * 16 : (fid * 32 + 8 * r) * 1024 + w4 * 256 + c * 16;
      const u32 step = stage ? FF * 2 : 1024;
#pragma unroll
      for (int n = 0; n < 8; ++n) W[n] = ld128<AUX_NT>(rw, voff, n * step);
    }
    L.ha[w4][2][lane] = 0x3f803f80u;
#pragma unroll
    for (int n = 0; n < 8; ++n)
#pragma unroll
      for (int jj2 = 0; jj2 < 4; ++jj2) B2[n][jj2] = __builtin_amdgcn_perm(W[2 * jj2 + 1][n >> 1], W[2 * jj2][n >> 1], (n & 1) ? 0x07060302u : 0x05040100u);
    asm volatile("" : "+v"(B2[0]), "+v"(B2[1]), "+v"(B2[2]), "+v"(B2[3]), "+v"(B2[4]), "+v"(B2[5]), "+v"(B2[6]), "+v"(B2[7]));
    if (stage) {
#pragma unroll
      for (int n = 0; n < 8; ++n) L.w1f[16 * w4 + (lane >> 2)][9 * (lane & 3) + n] = B2[n];
    }
    WG_BARRIER();
    if (stage) return;
    u32x4 B1[4][2];
#pragma unroll
    for (int ks = 0; ks < 4; ++ks) { B1[ks][0] = L.w1f[16 * w4 + 4 * ks + r][c + (c >> 3)]; B1[ks][1] = L.w1f[16 * w4 + 4 * ks + r][18 + c + (c >> 3)]; }
    asm volatile("" : "+v"(B1[0][0]), "+v"(B1[0][1]), "+v"(B1[1][0]), "+v"(B1[1][1]), "+v"(B1[2][0]), "+v"(B1[2][1]), "+v"(B1[3][0]), "+v"(B1[3][1]));
    const int bb = lane >> 5;
    lds_p a_hw = (lds_p)&L.ha[w4][lane & 1][2 * (lane >> 1)], a_hr = (lds_p)&L.ha[w4][c < 8 ? (c >> 2) : 2][4 * r];
    lds_p a_zw = (lds_p)&L.z[0][lane][w4], a_zr = (lds_p)L.z[r & 1][16 * bb + c], a_cr = (lds_p)L.z[r & 1][32 + c];
    lds_p a_qr = (lds_p)L.z[2][20 * bb], a_sr = (lds_p)L.z[2][32 + 4 * bb];
    lds_p a_gw = (lds_p)L.g[w4] + 2 * lane, a_gr = (lds_p)&L.g[w4][(c & 1) * 16 + 4 * r];
    u32 foff = WS_F + (fid * 512 + 2 * w4 * 64 + 4 * c + r) * 16;
    asm volatile("" : "+v"(a_hw), "+v"(a_hr), "+v"(a_zw), "+v"(a_zr), "+v"(a_cr), "+v"(a_qr), "+v"(a_sr), "+v"(a_gw), "+v"(a_gr), "+v"(foff));
    const u32x4 hv = poll_unit<PSLEEP>(rws, WS_H + (32 * w4 + (lane >> 1)) * 128 + (lane & 1) * 16, tag);
    LDS_AT(u32x2, a_hw, 0) = u32x2{hv[0], hv[2]};
    asm volatile("" ::: "memory");
    u32x4 a[4];
#pragma unroll
    for (int ks = 0; ks < 4; ++ks) a[ks] = LDS_AT(u32x4, a_hr, 64 * ks);
    SB();
    f32x4 d0 = f32x4{0.f, 0.f, 0.f, 0.f}, d1 = d0, dg = d0;
#pragma unroll
    for (int ks = 0; ks < 4; ++ks) {
      dg = mfma16(a[ks], a[ks], dg); d0 = mfma16(a[ks], B1[ks][0], d0); d1 = mfma16(a[ks], B1[ks][1], d1);
      SB();
    }
    LDS_AT(float, a_zw, 2048) = dg[0]; LDS_AT(float, a_zw, 0) = d0[0]; LDS_AT(float, a_zw, 1024) = d1[0];
    WG_BARRIER();
    {
      const f32x4 ss = LDS_AT(f32x4, a_sr, 0), qs = LDS_AT(f32x4, a_qr, 0), zs = LDS_AT(f32x4, a_zr, 0), cs = LDS_AT(f32x4, a_cr, 0);
      SB();
      const float m1 = sum4(ss) * (1.f / DM), m2 = sum4(qs) * (1.f / DM);
      const float mbv = rbf(m1), rdv = ln_rden(m1, m2);
      const float z = rbf(rbf(fmaf(-mbv, sum4(cs), sum4(zs)) * rdv) + asf(bcol));
      constexpr float C1 = -2.f * LOG2E * 0.7978845608028654f, C3 = C1 * 0.044715f;
      const float g = z * rcp(1.f + ex2(z * fmaf(z * z, C3, C1)));
      LDS_AT(u16, a_gw, 0) = (u16)pack_bf16(g, g);
      asm volatile("" ::: "memory");
      const u32x4 a2 = LDS_AT(u32x4, a_gr, 0);
      f32x4 y[8];
#pragma unroll
      for (int n = 0; n < 8; ++n) y[n] = mfma16(a2, B2[n], f32x4{0.f, 0.f, 0.f, 0.f});
      SB();
#pragma unroll
      for (int hh = 0; hh < 2; ++hh) {
        const float f0 = (r & 2) ? ((r & 1) ? y[4 * hh + 3][0] : y[4 * hh + 2][0]) : ((r & 1) ? y[4 * hh + 1][0] : y[4 * hh][0]);
        const float f1 = (r & 2) ? ((r & 1) ? y[4 * hh + 3][1] : y[4 * hh + 2][1]) : ((r & 1) ? y[4 * hh + 1][1] : y[4 * hh][1]);
        st_unit(rws, foff + hh * 1024, asu(f0), asu(f1), tag);
      }
    }
  }
  else {
    asm volatile("s_barrier" ::: "memory");
    const u32 pfv = ld32<0>(rp, poff, 0);
    asm volatile("" ::"v"(pfv));
  }
}

extern "C" void kernel_launch(void* const* d_in, const int* in_sizes, int n_in, void* d_out, int out_size, void* d_ws,
                              size_t ws_size, hipStream_t stream) {
  fused_block<<<256, 576, 0, stream>>>((const u16*)d_in[0], (const u16*)d_in[1], (const u16*)d_in[2], (const u16*)d_in[3],
                                       (const u16*)d_in[4], (const u16*)d_in[6], (const u16*)d_in[7], (const u16*)d_in[8],
                                       (const u16*)d_in[9], (u16*)d_out, (unsigned char*)d_ws);
}
```
